```python
import torch

def _softmax(x, axis=-1):
    return torch.softmax(x, dim=axis)

def _gelu(x):
    return torch.nn.functional.gelu(x, approximate='tanh')

def _silu(x):
    return torch.nn.functional.silu(x)

def _sigmoid(x):
    return torch.sigmoid(x)

def _layernorm(x, axis=-1, eps=1e-05):
    m = x.mean(dim=axis, keepdim=True)
    v = x.var(dim=axis, keepdim=True, unbiased=False)
    return (x - m) / torch.sqrt(v + eps)

def _scatter_add(tgt, idx, src, axis=0):
    return tgt.clone().index_add_(axis, idx, src)

def _segment_sum(src, seg, n):
    out = torch.zeros((n,) + src.shape[1:], dtype=src.dtype)
    return out.index_add_(0, seg, src)

def _pad(x, pw):
    flat = tuple((v for pair in reversed(pw) for v in pair))
    return torch.nn.functional.pad(x, flat)
kernel_name = 'adaLN_cond__spectral_fnet__dense_activation_relu__prompt'

def setup_inputs():

    _w = lambda a: (lambda t: t.to(torch.bfloat16) if t.is_floating_point() else t)(torch.as_tensor(a))

    def _A_setup():
        B, S, D = (1, 2048, 640)
        ns = torch.arange(S)[:, None] * torch.arange(S)[None, :]
        nd = torch.arange(D)[:, None] * torch.arange(D)[None, :]
        Ws_re = torch.cos(-2.0 * torch.pi * ns / S).to(float)
        Ws_im = torch.sin(-2.0 * torch.pi * ns / S).to(float)
        Wd_re = torch.cos(-2.0 * torch.pi * nd / D).to(float)
        Wd_im = torch.sin(-2.0 * torch.pi * nd / D).to(float)
        return {'x': _w((lambda *s: torch.randn(*s, dtype=torch.bfloat16) * 0.1)(B, S, D)), 'dft_seq_re': _w(Ws_re), 'dft_seq_im': _w(Ws_im), 'dft_hid_re': _w(Wd_re), 'dft_hid_im': _w(Wd_im)}
    _A_in = _A_setup()

    def _B_setup():
        B, S, D, Dff = (1, 2048, 640, 1536)
        return {'x': _w((lambda *s: torch.randn(*s, dtype=torch.bfloat16) * 0.1)(B, S, D)), 'w1': _w((lambda *s: torch.randn(*s, dtype=torch.bfloat16) * 3.2)(D, Dff)), 'w2': _w((lambda *s: torch.randn(*s, dtype=torch.bfloat16) * 3.2)(Dff, D))}
    _B_in = _B_setup()
    return {'A_x': _A_in['x'], 'A_dft_seq_re': _A_in['dft_seq_re'], 'A_dft_seq_im': _A_in['dft_seq_im'], 'A_dft_hid_re': _A_in['dft_hid_re'], 'A_dft_hid_im': _A_in['dft_hid_im'], 'B_x': _B_in['x'], 'B_w1': _B_in['w1'], 'B_w2': _B_in['w2'], 'c': _w((lambda *s: torch.randn(*s, dtype=torch.bfloat16) * 0.4)(1, 640) * 8.0), 'w_mod': _w((lambda *s: torch.randn(*s, dtype=torch.bfloat16) * 0.4)(640, 6 * 640) * 640 ** (-0.5) * 8.0)}

def reference(A_x, A_dft_seq_re, A_dft_seq_im, A_dft_hid_re, A_dft_hid_im, B_x, B_w1, B_w2, c, w_mod):

    def _A_ref(x, dft_seq_re, dft_seq_im, dft_hid_re, dft_hid_im):
        a = torch.einsum('bsd,ed->bse', x, dft_hid_re)
        b = torch.einsum('bsd,ed->bse', x, dft_hid_im)
        y = torch.einsum('ts,bse->bte', dft_seq_re, a) - torch.einsum('ts,bse->bte', dft_seq_im, b)
        return y

    def _B_ref(x, w1, w2):
        h = torch.einsum('bsd,df->bsf', x, w1)
        zero = torch.zeros_like(h)
        h = torch.maximum(h, zero)
        return torch.einsum('bsf,fd->bsd', h, w2)
    x = A_x
    mod = (_silu(c) @ w_mod).reshape(c.shape[0], 6, 1, -1)
    sa, ca, ga, sb, cb, gb = (mod[:, 0].clone(), mod[:, 1].clone(), mod[:, 2].clone(), mod[:, 3].clone(), mod[:, 4].clone(), mod[:, 5].clone())
    h = _layernorm(x, axis=-1) * (1 + ca) + sa
    x = x + ga * _A_ref(h, A_dft_seq_re, A_dft_seq_im, A_dft_hid_re, A_dft_hid_im)
    h = _layernorm(x, axis=-1) * (1 + cb) + sb
    x = x + gb * _B_ref(h, B_w1, B_w2)
    return x
```

```cpp
#include <hip/hip_runtime.h>
#include <hip/hip_bf16.h>
#include <stdint.h>

typedef __attribute__((ext_vector_type(8))) short bf16x8;
typedef __attribute__((ext_vector_type(4))) float f32x4;
typedef __attribute__((ext_vector_type(4))) unsigned u32x4;
typedef __attribute__((ext_vector_type(2))) unsigned u32x2;
typedef unsigned short u16;

constexpr int S_ = 2048, D_ = 640, F_ = 1536;

constexpr size_t WS_NT   = 0;
constexpr size_t WS_PQ   = WS_NT + (size_t)S_ * D_ * 2;
constexpr size_t WS_X1   = WS_PQ + (size_t)S_ * 2 * D_ * 2;
constexpr size_t WS_HH   = WS_X1 + (size_t)S_ * D_ * 2;
constexpr size_t WS_W1U  = WS_HH + (size_t)S_ * F_ * 2;
constexpr size_t WS_W2U  = WS_W1U + (size_t)D_ * F_ * 2;
constexpr size_t WS_MOD  = WS_W2U + (size_t)D_ * F_ * 2;

__device__ __forceinline__ float bf2f(u16 v) { return __uint_as_float(((unsigned)v) << 16); }
__device__ __forceinline__ float bflo(unsigned v) { return __uint_as_float(v << 16); }
__device__ __forceinline__ float bfhi(unsigned v) { return __uint_as_float(v & 0xffff0000u); }
__device__ __forceinline__ unsigned pk2(float a, float b) {
    __hip_bfloat162 h = __float22bfloat162_rn(make_float2(a, b));
    return *reinterpret_cast<unsigned*>(&h);
}
__device__ __forceinline__ float bfr(float a) { return bflo(pk2(a, 0.f) & 0xffffu); }

template <int V> struct IC { static constexpr int value = V; };
#define WAIT_V(n) asm volatile("s_waitcnt vmcnt(%0)" ::"n"(n) : "memory")
#define BAR_LGKM() asm volatile("s_waitcnt lgkmcnt(0)\n\ts_barrier" ::: "memory")
#define BAR_ONLY() asm volatile("s_barrier" ::: "memory")

__device__ __forceinline__ void glds16(const char* g, char* l) {
    __builtin_amdgcn_global_load_lds((const unsigned*)g, (unsigned*)l, 16, 0, 0);
}

__device__ __forceinline__ float modval(const float* __restrict__ part, int j) {
    float v = (part[j] + part[3840 + j]) + (part[2 * 3840 + j] + part[3 * 3840 + j]);
    return bfr(v);
}

constexpr int KA_LN = 64, KA_MOD = 240, KA_TR = 120;

__device__ __forceinline__ void retile_block(const u16* __restrict__ src, u16* __restrict__ dst, int N, int kb, int nb) {
    u32x4 r[8];
#pragma unroll
    for (int k = 0; k < 8; ++k) r[k] = *reinterpret_cast<const u32x4*>(src + (size_t)(8 * kb + k) * N + 8 * nb);
#pragma unroll
    for (int n = 0; n < 8; ++n) {
        u32x4 o;
#pragma unroll
        for (int p = 0; p < 4; ++p) {
            unsigned a = r[2 * p][n >> 1], b = r[2 * p + 1][n >> 1];
            o[p] = (n & 1) ? ((a >> 16) | (b & 0xffff0000u)) : ((a & 0xffffu) | (b << 16));
        }
        *reinterpret_cast<u32x4*>(dst + ((size_t)kb * N + 8 * nb + n) * 8) = o;
    }
}

__global__ __launch_bounds__(256) void ka_prep(const u16* __restrict__ x, const u16* __restrict__ c,
                                               const u16* __restrict__ wmod, const u16* __restrict__ w1,
                                               const u16* __restrict__ w2, char* __restrict__ ws) {
    __shared__ float red[4][64];
    const int tid = threadIdx.x, lane = tid & 63, wid = tid >> 6;
    const int bid = blockIdx.x;
    if (bid < KA_LN) {
        const int sb = bid * 4 + wid;
        const unsigned* xr = reinterpret_cast<const unsigned*>(x + (size_t)sb * 8 * D_);
        unsigned v[8][5];
#pragma unroll
        for (int r = 0; r < 8; ++r)
#pragma unroll
            for (int i = 0; i < 5; ++i) v[r][i] = xr[r * 320 + i * 64 + lane];
        float st[16];
        float sh[8];
#pragma unroll
        for (int r = 0; r < 8; ++r) {
            sh[r] = bflo(__builtin_amdgcn_readfirstlane(v[r][0]));
            float s = 0.f, q = 0.f;
#pragma unroll
            for (int i = 0; i < 5; ++i) {
                float a = bflo(v[r][i]) - sh[r], b = bfhi(v[r][i]) - sh[r];
                s += a + b;
                q += a * a + b * b;
            }
            st[r] = s;
            st[8 + r] = q;
        }
#pragma unroll
        for (int j = 0; j < 16; ++j) {
#pragma unroll
            for (int o = 32; o >= 1; o >>= 1) st[j] += __shfl_xor(st[j], o);
        }
        float mean[8], rstd[8];
#pragma unroll
        for (int r = 0; r < 8; ++r) {
            float m = st[r] * (1.f / D_);
            float var = fmaxf(st[8 + r] * (1.f / D_) - m * m, 0.f);
            mean[r] = m + sh[r];
            rstd[r] = 1.0f / sqrtf(var + 1e-5f);
        }
        u32x4* out = reinterpret_cast<u32x4*>(ws + WS_NT) + (size_t)sb * D_;
#pragma unroll
        for (int i = 0; i < 5; ++i) {
            u32x4 lo, hi;
#pragma unroll
            for (int p = 0; p < 4; ++p) {
                int r0 = 2 * p, r1 = 2 * p + 1;
                lo[p] = pk2((bflo(v[r0][i]) - mean[r0]) * rstd[r0], (bflo(v[r1][i]) - mean[r1]) * rstd[r1]);
                hi[p] = pk2((bfhi(v[r0][i]) - mean[r0]) * rstd[r0], (bfhi(v[r1][i]) - mean[r1]) * rstd[r1]);
            }
            int d = 2 * (i * 64 + lane);
            out[d] = lo;
            out[d + 1] = hi;
        }
    } else if (bid < KA_LN + KA_MOD) {
        const int b = bid - KA_LN, cg = b % 60, ks = b / 60;
        const int rg = tid >> 3, cl = tid & 7;
        u32x4 w[5];
        float sc[5];
#pragma unroll
        for (int p = 0; p < 5; ++p) {
            int row = ks * 160 + p * 32 + rg;
            w[p] = *reinterpret_cast<const u32x4*>(wmod + (size_t)row * 3840 + cg * 64 + cl * 8);
            float cv = bf2f(c[row]);
            sc[p] = bfr(cv / (1.f + __expf(-cv)));
        }
        float a[8];
#pragma unroll
        for (int j = 0; j < 8; ++j) a[j] = 0.f;
#pragma unroll
        for (int p = 0; p < 5; ++p)
#pragma unroll
            for (int j = 0; j < 4; ++j) {
                a[2 * j] += sc[p] * bflo(w[p][j]);
                a[2 * j + 1] += sc[p] * bfhi(w[p][j]);
            }
#pragma unroll
        for (int j = 0; j < 8; ++j) {
            a[j] += __shfl_xor(a[j], 8);
            a[j] += __shfl_xor(a[j], 16);
            a[j] += __shfl_xor(a[j], 32);
        }
        if (lane < 8) {
#pragma unroll
            for (int j = 0; j < 8; ++j) red[wid][lane * 8 + j] = a[j];
        }
        __syncthreads();
        if (tid < 64) {
            float s = (red[0][tid] + red[1][tid]) + (red[2][tid] + red[3][tid]);
            reinterpret_cast<float*>(ws + WS_MOD)[ks * 3840 + cg * 64 + tid] = s;
        }
    } else {
        int t = (bid - KA_LN - KA_MOD) * 256 + tid;
        if (t < 15360) {
            retile_block(w1, reinterpret_cast<u16*>(ws + WS_W1U), F_, t / 192, t % 192);
        } else {
            t -= 15360;
            retile_block(w2, reinterpret_cast<u16*>(ws + WS_W2U), D_, t / 80, t % 80);
        }
    }
}

template <int NT, int NS, int G, int J, class Issue>
__device__ __forceinline__ void loader_tail(Issue& issue) {
    if constexpr (J < NT) {
        constexpr int rem = (NT - 1 - (J + 1)) > 0 ? (NT - 1 - (J + 1)) : 0;
        WAIT_V(rem * G);
        BAR_ONLY();
        loader_tail<NT, NS, G, J + 1>(issue);
    }
}

template <int NT, int NS, int G, class Issue>
__device__ __forceinline__ void loader_loop(Issue& issue) {
#pragma unroll
    for (int i = 0; i < NS; ++i) issue(i);
    WAIT_V((NS - 1) * G);
    BAR_ONLY();
    for (int j = 0; j < NT - NS; ++j) {
        WAIT_V((NS - 2) * G);
        BAR_ONLY();
        issue(j + NS);
    }
    loader_tail<NT, NS, G, NT - NS>(issue);
}

template <int NT, class Read, class Mma>
__device__ __forceinline__ void compute_loop(Read& read, Mma& mma) {
    static_assert(NT % 2 == 0, "NT even");
    BAR_LGKM();
    read(0, 0);
    for (int j = 0; j < NT; j += 2) {
        BAR_LGKM();
        read(j + 1, 1);
        mma(0);
        BAR_LGKM();
        if (j + 2 < NT) read(j + 2, 0);
        mma(1);
    }
}

__device__ __forceinline__ void tile_coords(int bid, int& rb, int& cb) {
    rb = (bid & 7) * 4 + (bid >> 6);
    cb = (bid >> 3) & 7;
}

#define MFMA(a, b, c) __builtin_amdgcn_mfma_f32_16x16x32_bf16(a, b, c, 0, 0, 0)

constexpr int KB_NT = 32, KB_NS = 6, KB_STAGE = 16384 + 10240;
__global__ __launch_bounds__(512, 2) void kb_seq(const u16* __restrict__ wre, const u16* __restrict__ wim,
                                                 char* __restrict__ ws) {
    __shared__ __attribute__((aligned(1024))) char shm[KB_NS * KB_STAGE + 1024];
    float* modt = reinterpret_cast<float*>(shm + KB_NS * KB_STAGE);
    const int tid = threadIdx.x, lane = tid & 63;
    const int wid = __builtin_amdgcn_readfirstlane(tid >> 6);
    int rb, cb;
    tile_coords(blockIdx.x, rb, cb);
    const int t0 = rb * 64, n0 = cb * 80;
    if (wid >= 4) {
        const int lw = wid - 4;
        const char* abase = reinterpret_cast<const char*>(lw < 2 ? wre : wim);
        unsigned aoff[4], boff[3];
#pragma unroll
        for (int q = 0; q < 4; ++q) {
            int r = 32 * lw + 8 * q + (lane >> 3);
            int unit = (lane & 7) ^ ((r >> 1) & 7);
            aoff[q] = (unsigned)((t0 + (r & 63)) * 4096 + unit * 16);
        }
#pragma unroll
        for (int q = 0; q < 3; ++q) {
            int i = q * 64 + lane;
            int p = 2 * lw + (i >= 80 ? 1 : 0), row = i >= 80 ? i - 80 : i;
            boff[q] = (unsigned)((p * D_ + n0 + row) * 16);
        }
        const char* bbase = ws + WS_NT;
        auto issue = [&](int st) {
            char* sl = shm + (st % KB_NS) * KB_STAGE;
            const char* ag = abase + st * 128;
            const char* bg = bbase + (size_t)st * (8 * D_ * 16);
#pragma unroll
            for (int q = 0; q < 4; ++q) glds16(ag + aoff[q], sl + (32 * lw + 8 * q) * 128);
#pragma unroll
            for (int q = 0; q < 2; ++q) glds16(bg + boff[q], sl + 16384 + (160 * lw + 64 * q) * 16);
            if (lane < 32) glds16(bg + boff[2], sl + 16384 + (160 * lw + 128) * 16);
        };
        loader_loop<KB_NT, KB_NS, 7>(issue);
        return;
    }
    const int mh = wid & 1, kh = wid >> 1, fr = lane & 15, g = lane >> 4;
    if (tid < 160) {
        const float* part = reinterpret_cast<const float*>(ws + WS_MOD);
        int j = tid < 80 ? (640 + n0 + tid) : (n0 + tid - 80);
        float v = modval(part, j);
        modt[tid] = tid < 80 ? bfr(1.f + v) : v;
    }
    const int u = 4 * kh + g;
    const int moff = (64 * mh + fr) * 128 + ((u ^ (fr >> 1)) * 16);
    const int noff = 16384 + (u * 80 + fr) * 16;
    f32x4 acc[5][4], rs[4];
#pragma unroll
    for (int a = 0; a < 5; ++a)
#pragma unroll
        for (int b = 0; b < 4; ++b) acc[a][b] = f32x4{0.f, 0.f, 0.f, 0.f};
#pragma unroll
    for (int b = 0; b < 4; ++b) rs[b] = f32x4{0.f, 0.f, 0.f, 0.f};
    bf16x8 mf[2][4], nf[2][5];
    const short one = 0x3F80;
    const bf16x8 ones = {one, one, one, one, one, one, one, one};
    auto read = [&](int st, int s) {
        const char* sl = shm + (st % KB_NS) * KB_STAGE;
#pragma unroll
        for (int b = 0; b < 4; ++b) mf[s][b] = *reinterpret_cast<const bf16x8*>(sl + moff + b * 2048);
#pragma unroll
        for (int a = 0; a < 5; ++a) nf[s][a] = *reinterpret_cast<const bf16x8*>(sl + noff + a * 256);
    };
    auto mma = [&](int s) {
#pragma unroll
        for (int b = 0; b < 4; ++b) {
#pragma unroll
            for (int a = 0; a < 5; ++a) acc[a][b] = MFMA(nf[s][a], mf[s][b], acc[a][b]);
            rs[b] = MFMA(ones, mf[s][b], rs[b]);
        }
    };
    compute_loop<KB_NT>(read, mma);
    f32x4* xin = reinterpret_cast<f32x4*>(shm) + (size_t)wid * 12 * 64 + lane;
    f32x4* xout = reinterpret_cast<f32x4*>(shm) + (size_t)(wid ^ 2) * 12 * 64 + lane;
    char* pq = ws + WS_PQ;
    auto epi = [&](auto khc) {
        constexpr int KH = decltype(khc)::value;
#pragma unroll
        for (int b2 = 0; b2 < 2; ++b2) {
#pragma unroll
            for (int a = 0; a < 5; ++a) xout[(b2 * 6 + a) * 64] = acc[a][2 * (1 - KH) + b2];
            xout[(b2 * 6 + 5) * 64] = rs[2 * (1 - KH) + b2];
        }
        BAR_LGKM();
#pragma unroll
        for (int b2 = 0; b2 < 2; ++b2) {
            f32x4 r4 = xin[(b2 * 6 + 5) * 64];
            float rsum = rs[2 * KH + b2][0] + r4[0];
            int t = t0 + 16 * (2 * KH + b2) + fr;
#pragma unroll
            for (int a = 0; a < 5; ++a) {
                f32x4 o = xin[(b2 * 6 + a) * 64];
                f32x4 m = acc[a][2 * KH + b2];
                f32x4 c1 = *reinterpret_cast<const f32x4*>(modt + 16 * a + 4 * g);
                f32x4 s1 = *reinterpret_cast<const f32x4*>(modt + 80 + 16 * a + 4 * g);
                float vv[4];
#pragma unroll
                for (int r = 0; r < 4; ++r) {
                    float v = c1[r] * (m[r] + o[r]) + s1[r] * rsum;
                    vv[r] = mh ? -v : v;
                }
                int kp = 640 * mh + n0 + 16 * a + 4 * g;
                u32x2 st2 = {pk2(vv[0], vv[1]), pk2(vv[2], vv[3])};
                *reinterpret_cast<u32x2*>(pq + ((size_t)(kp >> 3) * S_ + t) * 16 + (kp & 7) * 2) = st2;
            }
        }
    };
    if (kh) epi(IC<1>{}); else epi(IC<0>{});
}

constexpr int KC_NS = 8, KC_STAGE = 8192 + 10240;
template <bool IS_KE>
__global__ __launch_bounds__(512, 2) void kc_out(const u16* __restrict__ wd_re, const u16* __restrict__ wd_im,
                                                 const u16* __restrict__ resid, u16* __restrict__ outp,
                                                 char* __restrict__ ws) {
    constexpr int NT = IS_KE ? 24 : 20;
    __shared__ __attribute__((aligned(1024))) char shm[KC_NS * KC_STAGE + 1024];
    float* modt = reinterpret_cast<float*>(shm + KC_NS * KC_STAGE);
    const int tid = threadIdx.x, lane = tid & 63;
    const int wid = __builtin_amdgcn_readfirstlane(tid >> 6);
    int rb, cb;
    tile_coords(blockIdx.x, rb, cb);
    const int t0 = rb * 64, n0 = cb * 80;
    if (wid >= 4) {
        const int lw = wid - 4;
        const char* abase = ws + (IS_KE ? WS_HH : WS_PQ);
        unsigned aoff[2], boff[3];
#pragma unroll
        for (int q = 0; q < 2; ++q) aoff[q] = (unsigned)(((2 * lw + q) * S_ + t0 + lane) * 16);
#pragma unroll
        for (int q = 0; q < 3; ++q) {
            if constexpr (IS_KE) {
                int i = q * 64 + lane;
                int p = 2 * lw + (i >= 80 ? 1 : 0), row = i >= 80 ? i - 80 : i;
                boff[q] = (unsigned)((p * D_ + n0 + row) * 16);
            } else {
                int r = 20 * lw + 8 * q + (lane >> 3);
                int unit = (lane & 7) ^ ((r >> 1) & 7);
                boff[q] = (unsigned)((n0 + r) * 1280 + unit * 16);
            }
        }
        auto issue = [&](int st) {
            char* sl = shm + (st % KC_NS) * KC_STAGE;
            const char* ag = abase + (size_t)st * (8 * S_ * 16);
            const char* bg;
            if constexpr (IS_KE) bg = ws + WS_W2U + (size_t)st * (8 * D_ * 16);
            else bg = st < 10 ? reinterpret_cast<const char*>(wd_re) + st * 128
                              : reinterpret_cast<const char*>(wd_im) + (st - 10) * 128;
#pragma unroll
            for (int q = 0; q < 2; ++q) glds16(ag + aoff[q], sl + (2 * lw + q) * 1024);
#pragma unroll
            for (int q = 0; q < 2; ++q) glds16(bg + boff[q], sl + 8192 + lw * 2560 + q * 1024);
            if (lane < 32) glds16(bg + boff[2], sl + 8192 + lw * 2560 + 2048);
        };
        loader_loop<NT, KC_NS, 5>(issue);
        return;
    }
    const int mh = wid & 1, kh = wid >> 1, fr = lane & 15, g = lane >> 4;
    if (tid < 80) {
        const float* part = reinterpret_cast<const float*>(ws + WS_MOD);
        modt[tid] = modval(part, (IS_KE ? 5 : 2) * 640 + n0 + tid);
    }
    const int trow = t0 + 32 * mh + 16 * kh + fr;
    u32x2 rx[5];
#pragma unroll
    for (int a = 0; a < 5; ++a)
        rx[a] = *reinterpret_cast<const u32x2*>(resid + (size_t)trow * D_ + n0 + 16 * a + 4 * g);
    const int u = 4 * kh + g;
    const int moff = (u * 64 + 32 * mh + fr) * 16;
    const int noff = IS_KE ? 8192 + (u * 80 + fr) * 16 : 8192 + fr * 128 + ((u ^ (fr >> 1)) * 16);
    constexpr int nstep = IS_KE ? 256 : 2048;
    f32x4 acc[5][2];
#pragma unroll
    for (int a = 0; a < 5; ++a)
#pragma unroll
        for (int b = 0; b < 2; ++b) acc[a][b] = f32x4{0.f, 0.f, 0.f, 0.f};
    bf16x8 mf[2][2], nf[2][5];
    auto read = [&](int st, int s) {
        const char* sl = shm + (st % KC_NS) * KC_STAGE;
#pragma unroll
        for (int b = 0; b < 2; ++b) mf[s][b] = *reinterpret_cast<const bf16x8*>(sl + moff + b * 256);
#pragma unroll
        for (int a = 0; a < 5; ++a) nf[s][a] = *reinterpret_cast<const bf16x8*>(sl + noff + a * nstep);
    };
    auto mma = [&](int s) {
#pragma unroll
        for (int b = 0; b < 2; ++b)
#pragma unroll
            for (int a = 0; a < 5; ++a) acc[a][b] = MFMA(nf[s][a], mf[s][b], acc[a][b]);
    };
    compute_loop<NT>(read, mma);
    f32x4* xin = reinterpret_cast<f32x4*>(shm) + (size_t)wid * 5 * 64 + lane;
    f32x4* xout = reinterpret_cast<f32x4*>(shm) + (size_t)(wid ^ 2) * 5 * 64 + lane;
    auto epi = [&](auto khc) {
        constexpr int KH = decltype(khc)::value;
#pragma unroll
        for (int a = 0; a < 5; ++a) xout[a * 64] = acc[a][1 - KH];
        BAR_LGKM();
#pragma unroll
        for (int a = 0; a < 5; ++a) {
            f32x4 o = xin[a * 64];
            f32x4 m = acc[a][KH];
            f32x4 g4 = *reinterpret_cast<const f32x4*>(modt + 16 * a + 4 * g);
            float xv[4] = {bflo(rx[a][0]), bfhi(rx[a][0]), bflo(rx[a][1]), bfhi(rx[a][1])};
            float vv[4];
#pragma unroll
            for (int r = 0; r < 4; ++r) vv[r] = xv[r] + bfr(g4[r] * bfr(m[r] + o[r]));
            u32x2 st2 = {pk2(vv[0], vv[1]), pk2(vv[2], vv[3])};
            *reinterpret_cast<u32x2*>(outp + (size_t)trow * D_ + n0 + 16 * a + 4 * g) = st2;
        }
    };
    if (kh) epi(IC<1>{}); else epi(IC<0>{});
}

constexpr int KD_NT = 10, KD_NS = 3, KD_STAGE = 192 * 128, KD_A = 80 * 1024;
__global__ __launch_bounds__(512, 2) void kd_ffn1(char* __restrict__ ws) {
    __shared__ __attribute__((aligned(1024))) char shm[KD_A + KD_NS * KD_STAGE + 5120];
    char* ring = shm + KD_A;
    float* modt = reinterpret_cast<float*>(shm + KD_A + KD_NS * KD_STAGE);
    const int tid = threadIdx.x, lane = tid & 63;
    const int wid = __builtin_amdgcn_readfirstlane(tid >> 6);
    int rb, cb;
    tile_coords(blockIdx.x, rb, cb);
    const int t0 = rb * 64, n0 = cb * 192;
    const int lw = wid - 4;
    unsigned boff[6];
    auto issue = [&](int st) {
        char* sl = ring + (st % KD_NS) * KD_STAGE;
        const char* bg = ws + WS_W1U + (size_t)st * (8 * F_ * 16);
#pragma unroll
        for (int q = 0; q < 6; ++q) glds16(bg + boff[q], sl + (lw * 6 + q) * 1024);
    };
    if (wid >= 4) {
#pragma unroll
        for (int q = 0; q < 6; ++q) {
            int i = (lw * 6 + q) * 64 + lane;
            int p = i / 192, row = i % 192;
            boff[q] = (unsigned)((p * F_ + n0 + row) * 16);
        }
#pragma unroll
        for (int i = 0; i < KD_NS; ++i) issue(i);
    }
    {
        const float* part = reinterpret_cast<const float*>(ws + WS_MOD);
        for (int j = tid; j < 1280; j += 512) {
            float v = modval(part, j < 640 ? 4 * 640 + j : 3 * 640 + (j - 640));
            modt[j] = j < 640 ? bfr(1.f + v) : v;
        }
        const int row = wid * 8 + (lane >> 3), cl = lane & 7;
        const u16* xr = reinterpret_cast<const u16*>(ws + WS_X1) + (size_t)(t0 + row) * D_;
        u32x4 v[10];
#pragma unroll
        for (int i = 0; i < 10; ++i) v[i] = *reinterpret_cast<const u32x4*>(xr + (cl + 8 * i) * 8);
        float s = 0.f;
#pragma unroll
        for (int i = 0; i < 10; ++i)
#pragma unroll
            for (int j = 0; j < 4; ++j) s += bflo(v[i][j]) + bfhi(v[i][j]);
        s += __shfl_xor(s, 1);
        s += __shfl_xor(s, 2);
        s += __shfl_xor(s, 4);
        const float mean = s * (1.f / D_);
        float q = 0.f;
#pragma unroll
        for (int i = 0; i < 10; ++i)
#pragma unroll
            for (int j = 0; j < 4; ++j) {
                float a = bflo(v[i][j]) - mean, b = bfhi(v[i][j]) - mean;
                q += a * a + b * b;
            }
        q += __shfl_xor(q, 1);
        q += __shfl_xor(q, 2);
        q += __shfl_xor(q, 4);
        const float rstd = 1.0f / sqrtf(q * (1.f / D_) + 1e-5f);
        asm volatile("s_waitcnt lgkmcnt(0)\n\ts_barrier" ::: "memory");
#pragma unroll
        for (int i = 0; i < 10; ++i) {
            const int un = cl + 8 * i;
            f32x4 c0 = *reinterpret_cast<const f32x4*>(modt + un * 8);
            f32x4 c1 = *reinterpret_cast<const f32x4*>(modt + un * 8 + 4);
            f32x4 s0 = *reinterpret_cast<const f32x4*>(modt + 640 + un * 8);
            f32x4 s1 = *reinterpret_cast<const f32x4*>(modt + 640 + un * 8 + 4);
            u32x4 o;
            o[0] = pk2((bflo(v[i][0]) - mean) * rstd * c0[0] + s0[0], (bfhi(v[i][0]) - mean) * rstd * c0[1] + s0[1]);
            o[1] = pk2((bflo(v[i][1]) - mean) * rstd * c0[2] + s0[2], (bfhi(v[i][1]) - mean) * rstd * c0[3] + s0[3]);
            o[2] = pk2((bflo(v[i][2]) - mean) * rstd * c1[0] + s1[0], (bfhi(v[i][2]) - mean) * rstd * c1[1] + s1[1]);
            o[3] = pk2((bflo(v[i][3]) - mean) * rstd * c1[2] + s1[2], (bfhi(v[i][3]) - mean) * rstd * c1[3] + s1[3]);
            *reinterpret_cast<u32x4*>(shm + un * 1024 + ((row ^ (un & 7)) * 16)) = o;
        }
    }
    if (wid >= 4) {
        asm volatile("s_waitcnt vmcnt(0) lgkmcnt(0)\n\ts_barrier" ::: "memory");
        for (int j = 0; j < KD_NT - KD_NS; ++j) {
            WAIT_V((KD_NS - 2) * 6);
            BAR_ONLY();
            issue(j + KD_NS);
        }
        loader_tail<KD_NT, KD_NS, 6, KD_NT - KD_NS>(issue);
        return;
    }
    const int nh = wid & 1, kh = wid >> 1, fr = lane & 15, g = lane >> 4;
    const int u = 4 * kh + g;
    const int moff = u * 1024 + ((fr ^ u) * 16);
    const int noff = (u * 192 + 96 * nh + fr) * 16;
    f32x4 acc[6][4];
#pragma unroll
    for (int a = 0; a < 6; ++a)
#pragma unroll
        for (int b = 0; b < 4; ++b) acc[a][b] = f32x4{0.f, 0.f, 0.f, 0.f};
    bf16x8 mf[2][4], nf[2][6];
    auto read = [&](int st, int s) {
        const char* sl = ring + (st % KD_NS) * KD_STAGE;
        const char* al = shm + st * 8192;
#pragma unroll
        for (int b = 0; b < 4; ++b) mf[s][b] = *reinterpret_cast<const bf16x8*>(al + moff + b * 256);
#pragma unroll
        for (int a = 0; a < 6; ++a) nf[s][a] = *reinterpret_cast<const bf16x8*>(sl + noff + a * 256);
    };
    auto mma = [&](int s) {
#pragma unroll
        for (int b = 0; b < 4; ++b)
#pragma unroll
            for (int a = 0; a < 6; ++a) acc[a][b] = MFMA(nf[s][a], mf[s][b], acc[a][b]);
    };
    compute_loop<KD_NT>(read, mma);
    f32x4* xin = reinterpret_cast<f32x4*>(shm) + (size_t)wid * 12 * 64 + lane;
    f32x4* xout = reinterpret_cast<f32x4*>(shm) + (size_t)(wid ^ 2) * 12 * 64 + lane;
    char* hh = ws + WS_HH;
    auto epi = [&](auto khc) {
        constexpr int KH = decltype(khc)::value;
#pragma unroll
        for (int b2 = 0; b2 < 2; ++b2)
#pragma unroll
            for (int a = 0; a < 6; ++a) xout[(b2 * 6 + a) * 64] = acc[a][2 * (1 - KH) + b2];
        BAR_LGKM();
#pragma unroll
        for (int b2 = 0; b2 < 2; ++b2) {
            int t = t0 + 16 * (2 * KH + b2) + fr;
#pragma unroll
            for (int a = 0; a < 6; ++a) {
                f32x4 o = xin[(b2 * 6 + a) * 64];
                f32x4 m = acc[a][2 * KH + b2];
                float vv[4];
#pragma unroll
                for (int r = 0; r < 4; ++r) vv[r] = fmaxf(m[r] + o[r], 0.f);
                int f = n0 + 96 * nh + 16 * a + 4 * g;
                u32x2 st2 = {pk2(vv[0], vv[1]), pk2(vv[2], vv[3])};
                *reinterpret_cast<u32x2*>(hh + ((size_t)(f >> 3) * S_ + t) * 16 + (f & 7) * 2) = st2;
            }
        }
    };
    if (kh) epi(IC<1>{}); else epi(IC<0>{});
}

extern "C" void kernel_launch(void* const* d_in, const int* in_sizes, int n_in, void* d_out, int out_size,
                              void* d_ws, size_t ws_size, hipStream_t stream) {
    const u16* x = (const u16*)d_in[0];
    const u16* wsre = (const u16*)d_in[1];
    const u16* wsim = (const u16*)d_in[2];
    const u16* wdre = (const u16*)d_in[3];
    const u16* wdim = (const u16*)d_in[4];
    const u16* w1 = (const u16*)d_in[6];
    const u16* w2 = (const u16*)d_in[7];
    const u16* c = (const u16*)d_in[8];
    const u16* wmod = (const u16*)d_in[9];
    char* ws = (char*)d_ws;
    u16* out = (u16*)d_out;
    ka_prep<<<KA_LN + KA_MOD + KA_TR, 256, 0, stream>>>(x, c, wmod, w1, w2, ws);
    kb_seq<<<256, 512, 0, stream>>>(wsre, wsim, ws);
    kc_out<false><<<256, 512, 0, stream>>>(wdre, wdim, x, (u16*)(ws + WS_X1), ws);
    kd_ffn1<<<256, 512, 0, stream>>>(ws);
    kc_out<true><<<256, 512, 0, stream>>>(nullptr, nullptr, (const u16*)(ws + WS_X1), out, ws);
}
```

```cpp
#include <hip/hip_runtime.h>
#include <hip/hip_bf16.h>
#include <stdint.h>

typedef __attribute__((ext_vector_type(8))) short bf16x8;
typedef __attribute__((ext_vector_type(4))) float f32x4;
typedef __attribute__((ext_vector_type(4))) unsigned u32x4;
typedef __attribute__((ext_vector_type(2))) unsigned u32x2;
typedef __attribute__((ext_vector_type(2))) __bf16 bf16x2_t;
typedef __attribute__((ext_vector_type(8))) __bf16 bf16x8_t;
typedef unsigned short u16;
typedef unsigned long long u64;

constexpr int S_ = 2048, D_ = 640, F_ = 1536;

constexpr unsigned WS_NT   = 0;
constexpr unsigned WS_PQ   = WS_NT + S_ * D_ * 2;
constexpr unsigned WS_X1   = WS_PQ + S_ * 2 * D_ * 2;
constexpr unsigned WS_HH   = WS_X1 + S_ * D_ * 2;
constexpr unsigned WS_W1U  = WS_HH + S_ * F_ * 2;
constexpr unsigned WS_W2U  = WS_W1U + D_ * F_ * 2;
constexpr unsigned WS_MOD  = WS_W2U + D_ * F_ * 2;
constexpr unsigned WS_RS   = WS_MOD + 65536;
constexpr unsigned WS_FG   = WS_RS + 2 * S_ * 4;
constexpr unsigned WS_END  = WS_FG + 3 * 32 * 32 * 8;

constexpr int L_KB_A = 0, L_KB_B = 65536;
constexpr int L_C_B = 0, L_C_A = 71680;
constexpr int L_D_A = 0, L_D_B = 81920;
constexpr int L_X = 106496;
constexpr int L_X_C = 0;
constexpr int L_MISC = 155648;
constexpr int L_TAB = 157184;
constexpr int T_CA = 0, T_SA = 80, T_GA = 160, T_GB = 240, T_CB = 320, T_SB = 960;
typedef __attribute__((ext_vector_type(4))) short s16x4;
typedef __attribute__((address_space(3))) s16x4* lds_s16x4_ptr;

__device__ __forceinline__ float bf2f(u16 v) { return __uint_as_float(((unsigned)v) << 16); }
__device__ __forceinline__ float bflo(unsigned v) { return __uint_as_float(v << 16); }
__device__ __forceinline__ float bfhi(unsigned v) { return __uint_as_float(v & 0xffff0000u); }
typedef __attribute__((ext_vector_type(2))) float f32x2;
__device__ __forceinline__ unsigned pk2v(f32x2 v) { return __builtin_bit_cast(unsigned, __builtin_convertvector(v, bf16x2_t)); }
__device__ __forceinline__ unsigned pk2(float a, float b) { return pk2v(f32x2{a, b}); }
__device__ __forceinline__ float bfr(float a) { return bflo(pk2(a, a)); }
__device__ __forceinline__ f32x2 up2(unsigned v) { return f32x2{bflo(v), bfhi(v)}; }
__device__ __forceinline__ f32x2 bfr2(f32x2 v) { return up2(pk2v(v)); }
template <int CTRL>
__device__ __forceinline__ float dpp_add(float v) {
    return v + __builtin_bit_cast(float, __builtin_amdgcn_update_dpp(0, __builtin_bit_cast(int, v), CTRL, 0xf, 0xf, true));
}
__device__ __forceinline__ float red8(float v) {
    v = dpp_add<0xB1>(v); v = dpp_add<0x4E>(v); v = dpp_add<0x141>(v);
    return v;
}
__device__ __forceinline__ float red32(float v) {
    v = red8(v); v = dpp_add<0x140>(v);
    auto r = __builtin_amdgcn_permlane16_swap(__builtin_bit_cast(unsigned, v), __builtin_bit_cast(unsigned, v), false, false);
    const unsigned r0 = r[0], r1 = r[1];
    return __uint_as_float(r0) + __uint_as_float(r1);
}

#define STAMP(idx)

#define SBAR() BAR_ONLY()
#define P0_A_SLEEP 8
#define P0_S_SLEEP 1
#define KB_CH(st) (((st) & ~7) | (((st) + 2 * rbl) & 7))
#define KB_CHA(st) KB_CH(st)
#define K0_ST(v, p) (*(p) = (v))
#define KB_B_AUX 0
#define WS_PF_NT true
#define BURST_NTE 11
#define BURST_J1 99
#define BURST_J2 99
#define BURST_WS0 0
#define BURST_WS1 0
#define BURST_NT0 0
#define BURST_NT1 0
#define BURST_JWD BURST_J2
#define KB_A_AUX 2
template <int V> struct IC { static constexpr int value = V; };
template <int N, int I = 0, class Fn>
__device__ __forceinline__ void static_for(Fn& f) {
    if constexpr (I < N) { f(IC<I>{}); static_for<N, I + 1>(f); }
}
#define WAIT_V(n) asm volatile("s_waitcnt vmcnt(%0)" ::"n"(n) : "memory")
#define BAR_ONLY() asm volatile("s_barrier" ::: "memory")
#define BAR_LGKM() do { __builtin_amdgcn_sched_barrier(0); asm volatile("" ::: "memory"); __builtin_amdgcn_s_waitcnt(0xC07F); \
    __builtin_amdgcn_s_barrier(); asm volatile("" ::: "memory"); __builtin_amdgcn_sched_barrier(0); } while (0)
#define MFMA(a, b, c) __builtin_amdgcn_mfma_f32_16x16x32_bf16(a, b, c, 0, 0, 0)
#define SGB(mask, n) __builtin_amdgcn_sched_group_barrier(mask, n, 0)

template <int AUX>
__device__ __forceinline__ void glds16(const char* g, char* l) {
    __builtin_amdgcn_global_load_lds((const unsigned*)g, (unsigned*)l, 16, 0, AUX);
}
template <bool NTP>
__device__ __forceinline__ void pf_line(const char* a, unsigned sink) {
    unsigned keep;
    if constexpr (NTP)
        asm volatile("s_mov_b32 %0, m0\n\ts_mov_b32 m0, %2\n\ts_nop 0\n\tglobal_load_lds_dword %1, off nt\n\ts_mov_b32 m0, %0"
                     : "=&s"(keep) : "v"(a), "s"(sink));
    else
        asm volatile("s_mov_b32 %0, m0\n\ts_mov_b32 m0, %2\n\ts_nop 0\n\tglobal_load_lds_dword %1, off\n\ts_mov_b32 m0, %0"
                     : "=&s"(keep) : "v"(a), "s"(sink));
}
__device__ __forceinline__ void flag_set(char* ws, unsigned off, u64 tag) {
    __hip_atomic_store(reinterpret_cast<u64*>(ws + off), tag, __ATOMIC_RELAXED, __HIP_MEMORY_SCOPE_AGENT);
}
__device__ __forceinline__ u64 flag_get(const char* ws, unsigned off) {
    return __hip_atomic_load(reinterpret_cast<const u64*>(ws + off), __ATOMIC_RELAXED, __HIP_MEMORY_SCOPE_AGENT);
}
template <int NPL>
__device__ __forceinline__ void poll_flags(const char* ws, unsigned off, int nflags, int lane, u64 tag) {
    for (int spin = 0; spin < (1 << 20); ++spin) {
        bool ok = true;
#pragma unroll
        for (int q = 0; q < NPL; ++q) {
            int i = q * 64 + lane;
            u64 v = flag_get(ws, off + (i < nflags ? i : 0) * 8);
            ok &= (v == tag);
        }
        if (__all(ok)) break;
        __builtin_amdgcn_s_sleep(2);
    }
    asm volatile("" ::: "memory");
}

template <int NT, int G, int J>
__device__ __forceinline__ void stream_tail() {
    if constexpr (J < NT) {
        constexpr int rem = (NT - 2 - J) > 0 ? (NT - 2 - J) : 0;
        WAIT_V(rem * G);
        BAR_ONLY();
        stream_tail<NT, G, J + 1>();
    }
}
struct NoHook { __device__ __forceinline__ void operator()(int) const {} };
template <int NT, int NSL, int G, class Issue, class Hook = NoHook>
__device__ __forceinline__ void stream_run(Issue& issue, Hook hook = Hook()) {
    WAIT_V((NSL - 1) * G);
    BAR_ONLY();
#pragma nounroll
    for (int j = 0; j < NT - NSL; ++j) {
        WAIT_V((NSL - 2) * G);
        BAR_ONLY();
        issue(j + NSL);
        hook(j);
    }
    stream_tail<NT, G, NT - NSL>();
}

template <int NT, class Read, class Mma, class Hook = NoHook>
__device__ __forceinline__ void compute_loop(Read& read, Mma& mma, Hook hook = Hook()) {
    BAR_LGKM();
    read(0, IC<0>{});
#pragma nounroll
    for (int j = 0; j < NT; j += 2) {
        BAR_LGKM();
        hook(j);
        read(j + 1, IC<1>{});
        mma(IC<0>{});
        BAR_LGKM();
        read(j + 2 < NT ? j + 2 : j, IC<0>{});
        mma(IC<1>{});
    }
}

__global__ __launch_bounds__(256) void k0_prep(const u16* __restrict__ x, const u16* __restrict__ w1,
                                               const u16* __restrict__ w2, const u16* __restrict__ cvec,
                                               const u16* __restrict__ wmod, char* __restrict__ ws) {
    __shared__ __attribute__((aligned(1024))) char shm[16384 + 10752 + 1024];
    const int tid = threadIdx.x, lane = tid & 63;
    const int wid = __builtin_amdgcn_readfirstlane(tid >> 6);
    const int bid = blockIdx.x;
    if (wid == 0) STAMP(20);
    if (tid < 12) reinterpret_cast<u64*>(ws + WS_FG)[bid * 12 + tid] = 0ull;
    const int row = 2 * wid + (lane >> 5), l32 = lane & 31;
    const u16* xr = x + (size_t)(bid * 8 + row) * D_;
    u32x2 v[5];
#pragma unroll
    for (int i = 0; i < 5; ++i) v[i] = __builtin_nontemporal_load(reinterpret_cast<const u32x2*>(xr + (l32 + 32 * i) * 4));
    const int cg = bid % 60, ks = bid / 60, rg = lane >> 3, cl = lane & 7;
    const int wt = bid * 2 + (wid - 2);
    const bool isw1 = wt < 240;
    const int wl = isw1 ? wt : wt - 240;
    u32x4 w[20];
    u16 cv[3];
    if (bid < 240) {
        if (wid == 1) {
#pragma unroll
            for (int p = 0; p < 3; ++p) { int r = p * 64 + lane; cv[p] = cvec[ks * 160 + (r < 160 ? r : 0)]; }
#pragma unroll
            for (int p = 0; p < 20; ++p)
                w[p] = __builtin_nontemporal_load(reinterpret_cast<const u32x4*>(wmod + (size_t)(ks * 160 + p * 8 + rg) * 3840 + cg * 64 + cl * 8));
        } else if (wid >= 2) {
            const int N = isw1 ? F_ : D_, t = wl * 64 + lane, nbn = isw1 ? 192 : 80;
            const int kb = t / nbn, nb = t % nbn;
            const u16* src = isw1 ? w1 : w2;
#pragma unroll
            for (int k = 0; k < 8; ++k)
                w[k] = __builtin_nontemporal_load(reinterpret_cast<const u32x4*>(src + (size_t)(8 * kb + k) * N + 8 * nb));
        }
    }
    const float c_lo = bflo(__builtin_amdgcn_readlane(v[0][0], 0)), c_hi = bflo(__builtin_amdgcn_readlane(v[0][0], 32));
    const float c0 = lane < 32 ? c_lo : c_hi;
    f32x2 d[10];
    f32x2 s1 = {0.f, 0.f}, s2 = {0.f, 0.f};
#pragma unroll
    for (int i = 0; i < 5; ++i)
#pragma unroll
        for (int j = 0; j < 2; ++j) {
            f32x2 e = up2(v[i][j]) - c0;
            d[2 * i + j] = e;
            s1 += e;
            s2 += e * e;
        }
    const float m1 = red32(s1[0] + s1[1]) * (1.f / D_);
    const float m2 = red32(s2[0] + s2[1]) * (1.f / D_);
    const float rstd = 1.0f / sqrtf(fmaxf(m2 - m1 * m1, 0.f) + 1e-5f);
    const float nb_ = -m1 * rstd;
    char* sc = shm + 16384;
#pragma unroll
    for (int i = 0; i < 5; ++i) {
        u32x2 o = {pk2v(d[2 * i] * rstd + nb_), pk2v(d[2 * i + 1] * rstd + nb_)};
        *reinterpret_cast<u32x2*>(sc + row * 1344 + (l32 + 32 * i) * 8) = o;
    }
    BAR_LGKM();
    const int tq = (lane >> 2) & 3, tp = lane & 3, tg = lane >> 4;
    auto tr_store = [&](int i) {
        const char* a0 = sc + tq * 1344 + (64 * i + 16 * tg + 4 * tp) * 2;
        s16x4 lo = __builtin_amdgcn_ds_read_tr16_b64_v4i16((lds_s16x4_ptr)(a0));
        s16x4 hi = __builtin_amdgcn_ds_read_tr16_b64_v4i16((lds_s16x4_ptr)(a0 + 4 * 1344));
        u32x2 l2 = __builtin_bit_cast(u32x2, lo), h2 = __builtin_bit_cast(u32x2, hi);
        K0_ST((u32x4{l2[0], l2[1], h2[0], h2[1]}), reinterpret_cast<u32x4*>(ws + WS_NT + (bid * D_ + 64 * i + lane) * 16));
    };
    if (wid == 0) {
#pragma unroll
        for (int i = 0; i < 7; ++i) tr_store(i);
    } else {
        tr_store(6 + wid);
    }
    if (bid < 240 && wid >= 1) {
        if (wid == 1) {
            float* sl = reinterpret_cast<float*>(shm + 16384 + 10752);
#pragma unroll
            for (int p = 0; p < 3; ++p) {
                float c1 = bf2f(cv[p]);
                sl[p * 64 + lane] = bfr(c1 / (1.f + __expf(-c1)));
            }
            f32x2 a[4];
#pragma unroll
            for (int j = 0; j < 4; ++j) a[j] = f32x2{0.f, 0.f};
#pragma unroll
            for (int p = 0; p < 20; ++p) {
                const float sv = sl[p * 8 + rg];
#pragma unroll
                for (int j = 0; j < 4; ++j) a[j] += up2(w[p][j]) * sv;
            }
            float r8[8] = {a[0][0], a[0][1], a[1][0], a[1][1], a[2][0], a[2][1], a[3][0], a[3][1]};
#pragma unroll
            for (int j = 0; j < 8; ++j) {
                r8[j] += __shfl_xor(r8[j], 8);
                r8[j] += __shfl_xor(r8[j], 16);
                r8[j] += __shfl_xor(r8[j], 32);
            }
            if (lane < 16) {
                f32x4 o = lane < 8 ? f32x4{r8[0], r8[1], r8[2], r8[3]} : f32x4{r8[4], r8[5], r8[6], r8[7]};
                *reinterpret_cast<f32x4*>(ws + WS_MOD + (ks * 3840 + cg * 64 + cl * 8 + (lane >> 3) * 4) * 4) = o;
            }
        } else {
            char* sr = shm + (wid - 2) * 8192;
#pragma unroll
            for (int n = 0; n < 8; ++n) {
                u32x4 o;
#pragma unroll
                for (int p = 0; p < 4; ++p) {
                    unsigned a = w[2 * p][n >> 1], b = w[2 * p + 1][n >> 1];
                    o[p] = (n & 1) ? ((a >> 16) | (b & 0xffff0000u)) : ((a & 0xffffu) | (b << 16));
                }
                *reinterpret_cast<u32x4*>(sr + lane * 128 + ((n ^ (lane & 7)) * 16)) = o;
            }
            char* ob = ws + (isw1 ? WS_W1U : WS_W2U) + (size_t)(wl * 512) * 16;
#pragma unroll
            for (int i = 0; i < 8; ++i) {
                u32x4 o = *reinterpret_cast<const u32x4*>(sr + (8 * i + (lane >> 3)) * 128 + (((lane & 7) ^ ((lane >> 3) & 7)) * 16));
                K0_ST(o, reinterpret_cast<u32x4*>(ob + (i * 64 + lane) * 16));
            }
        }
    }
    if (wid == 0) STAMP(21);
    if (wid == 1) STAMP(23);
    if (wid == 2) STAMP(24);
}

__global__ __launch_bounds__(512, 2) void k1_chain(const u16* __restrict__ x, const u16* __restrict__ wsre,
                                                   const u16* __restrict__ wsim, const u16* __restrict__ wdre,
                                                   const u16* __restrict__ wdim, u16* __restrict__ outp,
                                                   char* __restrict__ ws) {
    __shared__ __attribute__((aligned(1024))) char shm[163840];
    const int tid = threadIdx.x, lane = tid & 63;
    const int wid = __builtin_amdgcn_readfirstlane(tid >> 6);
    const int bid = blockIdx.x;
    const int rb = (bid & 7) * 4 + (bid >> 6), cb = (bid >> 3) & 7, rbl = bid >> 6;
    const int t0 = rb * 64, n0 = cb * 80;
    const auto wrs = __builtin_amdgcn_make_buffer_rsrc(ws, 0, WS_END, 0x00020000);
    const float* tab = reinterpret_cast<const float*>(shm + L_TAB);
    constexpr u64 tag = 1ull;
    int sidx = 0;
    const int mh = wid & 1, kh = (wid >> 1) & 1, fr = lane & 15, g = lane >> 4;
    const int u = 4 * kh + g;
    const int trow = t0 + 32 * mh + 16 * kh + fr;
    u32x2 rx[5];
    const unsigned sink = (unsigned)(size_t)(__attribute__((address_space(3))) char*)shm + L_MISC;

    if (wid >= 5) {
        if (wid < 7) {
            const int h = wid - 5;
            const char* abase = reinterpret_cast<const char*>(h ? wsim : wsre);
            unsigned aoff[8];
#pragma unroll
            for (int q = 0; q < 8; ++q) {
                int r = 8 * q + (lane >> 3);
                aoff[q] = (unsigned)((t0 + r) * 4096 + (((lane & 7) ^ ((r >> 1) & 7)) * 16));
            }
            auto issue = [&](int st) {
                char* sl = shm + L_KB_A + (st & 3) * 16384 + h * 8192;
                const char* ag = abase + KB_CHA(st) * 128;
#pragma unroll
                for (int q = 0; q < 8; ++q) glds16<KB_A_AUX>(ag + aoff[q], sl + q * 1024);
            };
            const int l6 = lane < 60 ? lane : 59;
            const char* pfa = h == 0 ? reinterpret_cast<const char*>(cb >= 4 ? wsim : wsre) + (t0 + ((16 * cb) & 63) + (lane & 15)) * 4096 + (lane >> 4) * 128
                                     : reinterpret_cast<const char*>(wdre) + (n0 + 20 * rbl + l6 % 20) * 1280 + (l6 / 20) * 128;
            if (h == 0) {
#pragma unroll
                for (int i = 0; i < BURST_WS0; ++i) pf_line<WS_PF_NT>(pfa + i * 512, sink);
            }
#pragma nounroll
            for (int i = 0; i < 4; ++i) issue(i);
            auto hook = [&](int j) {
                if (h == 0) {
                    if (j == BURST_J1) {
#pragma unroll
                        for (int i = BURST_WS0; i < BURST_WS1; ++i) pf_line<true>(pfa + i * 512, sink);
                    } else if (j == BURST_J2) {
#pragma unroll
                        for (int i = BURST_WS1; i < 8; ++i) pf_line<true>(pfa + i * 512, sink);
                    }
                } else if (j == BURST_JWD) {
                    const long dw = reinterpret_cast<const char*>(wdim) - reinterpret_cast<const char*>(wdre);
#pragma unroll
                    for (int k = 0; k < 8; ++k)
                        if ((k & 3) < 3 || lane < 20) pf_line<true>(pfa + (k >= 4 ? dw : 0) + (k & 3) * 384, sink);
                }
            };
            stream_run<32, 4, 8>(issue, hook);
        } else {
            unsigned boff[10];
#pragma unroll
            for (int q = 0; q < 10; ++q) {
                int i = q * 64 + lane;
                boff[q] = (unsigned)(WS_NT + ((i / 80) * D_ + n0 + (i % 80)) * 16);
            }
            auto issue = [&](int st) {
                char* sl = shm + L_KB_B + (st & 3) * 10240;
                const char* bg = ws + KB_CH(st) * (8 * D_ * 16);
#pragma unroll
                for (int q = 0; q < 10; ++q) glds16<KB_B_AUX>(bg + boff[q], sl + q * 1024);
            };
            const int l6 = lane < 60 ? lane : 59, r = l6 % 20;
            const char* pfa = ws + WS_NT + ((8 * (l6 / 20) + 2 * rbl + r / 10) * D_ + n0) * 16 + (r % 10) * 128;
#pragma unroll
            for (int i = 0; i < BURST_NT0; ++i) pf_line<false>(pfa + i * (3 * 8 * D_ * 16), sink);
#pragma nounroll
            for (int i = 0; i < 4; ++i) issue(i);
            auto hook = [&](int j) {
                if (j == BURST_J1) {
#pragma unroll
                    for (int i = BURST_NT0; i < BURST_NT1; ++i) pf_line<false>(pfa + i * (3 * 8 * D_ * 16), sink);
                } else if (j == BURST_J2) {
#pragma unroll
                    for (int i = BURST_NT1; i < BURST_NTE; ++i)
                        if (i < 10 || lane < 40) pf_line<false>(pfa + i * (3 * 8 * D_ * 16), sink);
                }
            };
            stream_run<32, 4, 10>(issue, hook);
        }
        if (wid == 5) {
            unsigned aoff[8];
#pragma unroll
            for (int q = 0; q < 8; ++q) aoff[q] = (unsigned)(WS_PQ + (q * S_ + t0 + lane) * 16);
            auto issue = [&](int st) {
                char* sl = shm + L_C_A + (st & 3) * 8192;
                int ch = st + 5 * rbl;
                ch = ch >= 20 ? ch - 20 : ch;
                const char* ag = ws + ch * (8 * S_ * 16);
#pragma unroll
                for (int q = 0; q < 8; ++q) glds16<16>(ag + aoff[q], sl + q * 1024);
            };
            BAR_ONLY();
            BAR_ONLY();
            poll_flags<1>(ws, WS_FG + (0 * 32 + rb) * 256, 32, lane, tag);
            STAMP(9);
#pragma nounroll
            for (int i = 0; i < 4; ++i) issue(i);
            stream_run<20, 4, 8>(issue);
        } else {
            const int h = wid - 6;
            unsigned boff[5];
#pragma unroll
            for (int q = 0; q < 5; ++q) {
                int r = 40 * h + 8 * q + (lane >> 3);
                boff[q] = (unsigned)((n0 + r) * 1280 + (((lane & 7) ^ ((r >> 1) & 7)) * 16));
            }
            auto issue = [&](int st) {
                char* sl = shm + L_C_B + (st % 7) * 10240 + h * 5120;
                int ch = st + 5 * rbl;
                ch = ch >= 20 ? ch - 20 : ch;
                const char* bg = ch < 10 ? reinterpret_cast<const char*>(wdre) + ch * 128
                                         : reinterpret_cast<const char*>(wdim) + (ch - 10) * 128;
#pragma unroll
                for (int q = 0; q < 5; ++q) glds16<0>(bg + boff[q], sl + q * 1024);
            };
            BAR_ONLY();
            BAR_ONLY();
#pragma nounroll
            for (int i = 0; i < 7; ++i) issue(i);
            stream_run<20, 7, 5>(issue);
        }
    } else if (wid == 4) {
        {
            const float* part = reinterpret_cast<const float*>(ws + WS_MOD);
            f32x4 p[7][4];
#pragma unroll
            for (int it = 0; it < 7; ++it) {
                int qd = it * 64 + lane;
                qd = qd < 400 ? qd : 399;
                int col = qd < 20 ? 640 + n0 + qd * 4 : qd < 40 ? n0 + (qd - 20) * 4 : qd < 60 ? 1280 + n0 + (qd - 40) * 4
                        : qd < 80 ? 3200 + n0 + (qd - 60) * 4 : qd < 240 ? 2560 + (qd - 80) * 4 : 1920 + (qd - 240) * 4;
#pragma unroll
                for (int k = 0; k < 4; ++k) p[it][k] = *reinterpret_cast<const f32x4*>(part + k * 3840 + col);
            }
            auto step = [&](auto jc) {
                constexpr int it = decltype(jc)::value;
                int qd = it * 64 + lane;
                qd = qd < 400 ? qd : 399;
                const bool addone = qd < 20 || (qd >= 80 && qd < 240);
                f32x4 o;
#pragma unroll
                for (int r = 0; r < 4; ++r) {
                    float m = bfr((p[it][0][r] + p[it][1][r]) + (p[it][2][r] + p[it][3][r]));
                    o[r] = bfr(addone ? 1.f + m : m);
                }
                *reinterpret_cast<f32x4*>(shm + L_TAB + qd * 16) = o;
            };
            static_for<7>(step);
            asm volatile("s_waitcnt lgkmcnt(0)" ::: "memory");
        }
#pragma nounroll
        for (int j = 0; j < 33; ++j) SBAR();
        SBAR();
        BAR_ONLY();
#pragma nounroll
        for (int j = 0; j < 21; ++j) SBAR();
    } else {
        STAMP(0 + (wid ? 16 + wid : 0));
#pragma unroll
        for (int a = 0; a < 5; ++a)
            rx[a] = *reinterpret_cast<const u32x2*>(x + (size_t)trow * D_ + n0 + 16 * a + 4 * g);

        {
            const int moff = L_KB_A + (64 * mh + fr) * 128 + ((u ^ (fr >> 1)) * 16);
            const int noff = L_KB_B + (u * 80 + fr) * 16;
            f32x4 acc[6][4];
#pragma unroll
            for (int a = 0; a < 6; ++a)
#pragma unroll
                for (int b = 0; b < 4; ++b) acc[a][b] = f32x4{0.f, 0.f, 0.f, 0.f};
            bf16x8 mf[2][4], nf[2][5];
            const short one = 0x3F80;
            const bf16x8 ones = {one, one, one, one, one, one, one, one};
            auto read = [&](int st, auto sc_) {
                constexpr int s = decltype(sc_)::value;
                const char* al = shm + moff + (st & 3) * 16384;
                const char* bl = shm + noff + (st & 3) * 10240;
#pragma unroll
                for (int b = 0; b < 4; ++b) mf[s][b] = *reinterpret_cast<const bf16x8*>(al + b * 2048);
#pragma unroll
                for (int a = 0; a < 5; ++a) nf[s][a] = *reinterpret_cast<const bf16x8*>(bl + a * 256);
            };
            auto mma = [&](auto sc_) {
                constexpr int s = decltype(sc_)::value;
#pragma unroll
                for (int b = 0; b < 4; ++b) {
#pragma unroll
                    for (int a = 0; a < 5; ++a) acc[a][b] = MFMA(nf[s][a], mf[s][b], acc[a][b]);
                    acc[5][b] = MFMA(ones, mf[s][b], acc[5][b]);
                }
#pragma unroll
                for (int i = 0; i < 9; ++i) { SGB(0x8, 2); SGB(0x100, 1); }
                SGB(0x8, 6);
            };
            compute_loop<32>(read, mma);
            if (wid == 0) STAMP(2);
            f32x4* xin = reinterpret_cast<f32x4*>(shm + L_X) + wid * 768 + lane;
            f32x4* xout = reinterpret_cast<f32x4*>(shm + L_X) + (wid ^ 2) * 768 + lane;
            const unsigned sgn = mh ? 0x80008000u : 0u;
            auto epi = [&](auto khc) {
                constexpr int KH = decltype(khc)::value;
#pragma unroll
                for (int b2 = 0; b2 < 2; ++b2)
#pragma unroll
                    for (int a = 0; a < 6; ++a) xout[(b2 * 6 + a) * 64] = acc[a][2 * (1 - KH) + b2];
                BAR_LGKM();
#pragma unroll
                for (int b2 = 0; b2 < 2; ++b2) {
                    const float rsum = acc[5][2 * KH + b2][0] + xin[(b2 * 6 + 5) * 64][0];
                    int t = t0 + 16 * (2 * KH + b2) + fr;
#pragma unroll
                    for (int a = 0; a < 5; ++a) {
                        f32x4 o = xin[(b2 * 6 + a) * 64];
                        f32x4 m = acc[a][2 * KH + b2];
                        f32x4 cc = *reinterpret_cast<const f32x4*>(tab + T_CA + 16 * a + 4 * g);
                        f32x4 ss = *reinterpret_cast<const f32x4*>(tab + T_SA + 16 * a + 4 * g);
                        f32x4 vv = cc * (m + o) + ss * rsum;
                        int kp = 640 * mh + n0 + 16 * a + 4 * g;
                        u32x2 st2 = {pk2(vv[0], vv[1]) ^ sgn, pk2(vv[2], vv[3]) ^ sgn};
                        __builtin_amdgcn_raw_buffer_store_b64(st2, wrs, WS_PQ + ((kp >> 3) * S_ + t) * 16 + (kp & 7) * 2, 0, 16);
                    }
                }
            };
            if (kh) epi(IC<1>{}); else epi(IC<0>{});
            WAIT_V(0);
            if (lane == 0) flag_set(ws, WS_FG + (0 * 32 + rb) * 256 + (cb * 4 + wid) * 8, tag);
            if (wid == 0) STAMP(3);
            BAR_ONLY();
        }
        {
            const int moff = L_C_A + (u * 64 + 32 * mh + fr) * 16;
            const int noff = L_C_B + fr * 128 + ((u ^ (fr >> 1)) * 16);
            f32x4 acc[5][2];
#pragma unroll
            for (int a = 0; a < 5; ++a)
#pragma unroll
                for (int b = 0; b < 2; ++b) acc[a][b] = f32x4{0.f, 0.f, 0.f, 0.f};
            bf16x8 mf[2][2], nf[2][5];
            auto read = [&](int st, auto sc_) {
                constexpr int s = decltype(sc_)::value;
                const char* al = shm + moff + (st & 3) * 8192;
                const char* bl = shm + noff + (st % 7) * 10240;
#pragma unroll
                for (int b = 0; b < 2; ++b) mf[s][b] = *reinterpret_cast<const bf16x8*>(al + b * 256);
#pragma unroll
                for (int a = 0; a < 5; ++a) nf[s][a] = *reinterpret_cast<const bf16x8*>(bl + a * 2048);
            };
            auto mma = [&](auto sc_) {
                constexpr int s = decltype(sc_)::value;
#pragma unroll
                for (int b = 0; b < 2; ++b)
#pragma unroll
                    for (int a = 0; a < 5; ++a) acc[a][b] = MFMA(nf[s][a], mf[s][b], acc[a][b]);
#pragma unroll
                for (int i = 0; i < 7; ++i) { SGB(0x8, 1); SGB(0x100, 1); }
                SGB(0x8, 3);
            };
            compute_loop<20>(read, mma);
            if (wid == 0) STAMP(4);
            f32x4* xin = reinterpret_cast<f32x4*>(shm + L_X_C) + wid * 320 + lane;
            f32x4* xout = reinterpret_cast<f32x4*>(shm + L_X_C) + (wid ^ 2) * 320 + lane;
            auto epi = [&](auto khc) {
                constexpr int KH = decltype(khc)::value;
#pragma unroll
                for (int a = 0; a < 5; ++a) xout[a * 64] = acc[a][1 - KH];
                BAR_LGKM();
#pragma unroll
                for (int a = 0; a < 5; ++a) {
                    f32x4 o = xin[a * 64];
                    f32x4 m = acc[a][KH];
                    f32x4 gg = *reinterpret_cast<const f32x4*>(tab + T_GA + 16 * a + 4 * g);
                    f32x4 y = m + o;
                    f32x2 v0 = up2(rx[a][0]) + bfr2(f32x2{gg[0], gg[1]} * bfr2(f32x2{y[0], y[1]}));
                    f32x2 v1 = up2(rx[a][1]) + bfr2(f32x2{gg[2], gg[3]} * bfr2(f32x2{y[2], y[3]}));
                    float vv[4] = {v0[0], v0[1], v1[0], v1[1]};
                    rx[a] = u32x2{pk2(vv[0], vv[1]), pk2(vv[2], vv[3])};
                    __builtin_amdgcn_raw_buffer_store_b64(rx[a], wrs, WS_X1 + (trow * D_ + n0 + 16 * a + 4 * g) * 2, 0, 16);
                }
            };
            if (kh) epi(IC<1>{}); else epi(IC<0>{});
            WAIT_V(0);
            if (lane == 0) flag_set(ws, WS_FG + (1 * 32 + rb) * 256 + (cb * 4 + wid) * 8, tag);
            if (wid == 0) STAMP(5);
            BAR_ONLY();
        }
    }

    const int lw = wid - 5;
    unsigned doff[8];
#pragma unroll
    for (int q = 0; q < 8; ++q) {
        int i = (lw * 8 + q) * 64 + lane;
        doff[q] = (unsigned)(WS_W1U + ((i / 192) * F_ + cb * 192 + (i % 192)) * 16);
    }
    auto issue_d = [&](int st) {
        char* sl = shm + L_D_B + (st % 3) * 24576 + lw * 8192;
        int ch = st + (5 * rbl) / 2;
        ch = ch >= 10 ? ch - 10 : ch;
        const char* bg = ws + ch * (8 * F_ * 16);
#pragma unroll
        for (int q = 0; q < 8; ++q) glds16<0>(bg + doff[q], sl + q * 1024);
    };
    if (wid >= 4) {
        if (wid == 4) SBAR(); else BAR_ONLY();
        BAR_ONLY();
        if (wid >= 5) { issue_d(0); issue_d(1); issue_d(2); }
        if (wid == 4) {
            poll_flags<1>(ws, WS_FG + (1 * 32 + rb) * 256, 32, lane, tag);
            STAMP(10);
        }
    }
    if (wid == 4) SBAR(); else BAR_ONLY();
    {
        const int row = wid * 8 + (lane >> 3), cl = lane & 7;
        u32x4 v[10];
#pragma unroll
        for (int i = 0; i < 10; ++i)
            v[i] = __builtin_amdgcn_raw_buffer_load_b128(wrs, WS_X1 + ((t0 + row) * D_ + (cl + 8 * i) * 8) * 2, 0, 16);
        f32x2 d[40];
        f32x2 s1 = {0.f, 0.f};
#pragma unroll
        for (int i = 0; i < 10; ++i)
#pragma unroll
            for (int j = 0; j < 4; ++j) { d[4 * i + j] = up2(v[i][j]); s1 += d[4 * i + j]; }
        const float mean = red8(s1[0] + s1[1]) * (1.f / D_);
        f32x2 s2 = {0.f, 0.f};
#pragma unroll
        for (int i = 0; i < 40; ++i) { d[i] -= mean; s2 += d[i] * d[i]; }
        const float rstd = 1.0f / sqrtf(red8(s2[0] + s2[1]) * (1.f / D_) + 1e-5f);
#pragma unroll
        for (int i = 0; i < 10; ++i) {
            const int un = cl + 8 * i;
            f32x4 c0 = *reinterpret_cast<const f32x4*>(tab + T_CB + un * 8), c1 = *reinterpret_cast<const f32x4*>(tab + T_CB + un * 8 + 4);
            f32x4 t0_ = *reinterpret_cast<const f32x4*>(tab + T_SB + un * 8), t1_ = *reinterpret_cast<const f32x4*>(tab + T_SB + un * 8 + 4);
            u32x4 o;
            o[0] = pk2v(d[4 * i + 0] * (f32x2{c0[0], c0[1]} * rstd) + f32x2{t0_[0], t0_[1]});
            o[1] = pk2v(d[4 * i + 1] * (f32x2{c0[2], c0[3]} * rstd) + f32x2{t0_[2], t0_[3]});
            o[2] = pk2v(d[4 * i + 2] * (f32x2{c1[0], c1[1]} * rstd) + f32x2{t1_[0], t1_[1]});
            o[3] = pk2v(d[4 * i + 3] * (f32x2{c1[2], c1[3]} * rstd) + f32x2{t1_[2], t1_[3]});
            *reinterpret_cast<u32x4*>(shm + L_D_A + un * 1024 + ((row ^ (un & 7)) * 16)) = o;
        }
        asm volatile("s_waitcnt vmcnt(0) lgkmcnt(0)" ::: "memory");
    }

    if (wid >= 5) {
        stream_run<10, 3, 8>(issue_d);
        if (wid == 5) {
            unsigned aoff[8];
#pragma unroll
            for (int q = 0; q < 8; ++q) aoff[q] = (unsigned)(WS_HH + (q * S_ + t0 + lane) * 16);
            auto issue = [&](int st) {
                char* sl = shm + L_C_A + (st & 3) * 8192;
                int ch = st + 6 * rbl;
                ch = ch >= 24 ? ch - 24 : ch;
                const char* ag = ws + ch * (8 * S_ * 16);
#pragma unroll
                for (int q = 0; q < 8; ++q) glds16<16>(ag + aoff[q], sl + q * 1024);
            };
            BAR_ONLY();
            BAR_ONLY();
            poll_flags<1>(ws, WS_FG + (2 * 32 + rb) * 256, 32, lane, tag);
            STAMP(11);
#pragma nounroll
            for (int i = 0; i < 4; ++i) issue(i);
            stream_run<24, 4, 8>(issue);
        } else {
            const int h = wid - 6;
            unsigned boff[5];
#pragma unroll
            for (int q = 0; q < 5; ++q) {
                int i = (h * 5 + q) * 64 + lane;
                boff[q] = (unsigned)(WS_W2U + ((i / 80) * D_ + n0 + (i % 80)) * 16);
            }
            auto issue = [&](int st) {
                char* sl = shm + L_C_B + (st % 7) * 10240 + h * 5120;
                int ch = st + 6 * rbl;
                ch = ch >= 24 ? ch - 24 : ch;
                const char* bg = ws + ch * (8 * D_ * 16);
#pragma unroll
                for (int q = 0; q < 5; ++q) glds16<0>(bg + boff[q], sl + q * 1024);
            };
            BAR_ONLY();
            BAR_ONLY();
#pragma nounroll
            for (int i = 0; i < 7; ++i) issue(i);
            stream_run<24, 7, 5>(issue);
        }
    } else if (wid == 4) {
#pragma nounroll
        for (int j = 0; j < 12; ++j) SBAR();
    } else {
        {
            const int nh = mh;
            const int moff = L_D_A + u * 1024 + ((fr ^ u) * 16);
            const int noff = L_D_B + (u * 192 + 96 * nh + fr) * 16;
            f32x4 acc[6][4];
#pragma unroll
            for (int a = 0; a < 6; ++a)
#pragma unroll
                for (int b = 0; b < 4; ++b) acc[a][b] = f32x4{0.f, 0.f, 0.f, 0.f};
            bf16x8 mf[2][4], nf[2][6];
            auto read = [&](int st, auto sc_) {
                constexpr int s = decltype(sc_)::value;
                int ch = st + (5 * rbl) / 2;
                ch = ch >= 10 ? ch - 10 : ch;
                const char* al = shm + moff + ch * 8192;
                const char* bl = shm + noff + (st % 3) * 24576;
#pragma unroll
                for (int b = 0; b < 4; ++b) mf[s][b] = *reinterpret_cast<const bf16x8*>(al + b * 256);
#pragma unroll
                for (int a = 0; a < 6; ++a) nf[s][a] = *reinterpret_cast<const bf16x8*>(bl + a * 256);
            };
            auto mma = [&](auto sc_) {
                constexpr int s = decltype(sc_)::value;
#pragma unroll
                for (int b = 0; b < 4; ++b)
#pragma unroll
                    for (int a = 0; a < 6; ++a) acc[a][b] = MFMA(nf[s][a], mf[s][b], acc[a][b]);
#pragma unroll
                for (int i = 0; i < 10; ++i) { SGB(0x8, 2); SGB(0x100, 1); }
                SGB(0x8, 4);
            };
            compute_loop<10>(read, mma);
            if (wid == 0) STAMP(6);
            f32x4* xin = reinterpret_cast<f32x4*>(shm + L_X) + wid * 768 + lane;
            f32x4* xout = reinterpret_cast<f32x4*>(shm + L_X) + (wid ^ 2) * 768 + lane;
            auto epi = [&](auto khc) {
                constexpr int KH = decltype(khc)::value;
#pragma unroll
                for (int b2 = 0; b2 < 2; ++b2)
#pragma unroll
                    for (int a = 0; a < 6; ++a) xout[(b2 * 6 + a) * 64] = acc[a][2 * (1 - KH) + b2];
                BAR_LGKM();
#pragma unroll
                for (int b2 = 0; b2 < 2; ++b2) {
                    int t = t0 + 16 * (2 * KH + b2) + fr;
#pragma unroll
                    for (int a = 0; a < 6; ++a) {
                        f32x4 o = xin[(b2 * 6 + a) * 64];
                        f32x4 m = acc[a][2 * KH + b2];
                        float vv[4];
#pragma unroll
                        for (int r = 0; r < 4; ++r) vv[r] = fmaxf(m[r] + o[r], 0.f);
                        int f = cb * 192 + 96 * nh + 16 * a + 4 * g;
                        u32x2 st2 = {pk2(vv[0], vv[1]), pk2(vv[2], vv[3])};
                        __builtin_amdgcn_raw_buffer_store_b64(st2, wrs, WS_HH + ((f >> 3) * S_ + t) * 16 + (f & 7) * 2, 0, 16);
                    }
                }
            };
            if (kh) epi(IC<1>{}); else epi(IC<0>{});
            WAIT_V(0);
            if (lane == 0) flag_set(ws, WS_FG + (2 * 32 + rb) * 256 + (cb * 4 + wid) * 8, tag);
            if (wid == 0) STAMP(7);
            BAR_ONLY();
        }
        {
            const int moff = L_C_A + (u * 64 + 32 * mh + fr) * 16;
            const int noff = L_C_B + (u * 80 + fr) * 16;
            f32x4 acc[5][2];
#pragma unroll
            for (int a = 0; a < 5; ++a)
#pragma unroll
                for (int b = 0; b < 2; ++b) acc[a][b] = f32x4{0.f, 0.f, 0.f, 0.f};
            bf16x8 mf[2][2], nf[2][5];
            auto read = [&](int st, auto sc_) {
                constexpr int s = decltype(sc_)::value;
                const char* al = shm + moff + (st & 3) * 8192;
                const char* bl = shm + noff + (st % 7) * 10240;
#pragma unroll
                for (int b = 0; b < 2; ++b) mf[s][b] = *reinterpret_cast<const bf16x8*>(al + b * 256);
#pragma unroll
                for (int a = 0; a < 5; ++a) nf[s][a] = *reinterpret_cast<const bf16x8*>(bl + a * 256);
            };
            auto mma = [&](auto sc_) {
                constexpr int s = decltype(sc_)::value;
#pragma unroll
                for (int b = 0; b < 2; ++b)
#pragma unroll
                    for (int a = 0; a < 5; ++a) acc[a][b] = MFMA(nf[s][a], mf[s][b], acc[a][b]);
#pragma unroll
                for (int i = 0; i < 7; ++i) { SGB(0x8, 1); SGB(0x100, 1); }
                SGB(0x8, 3);
            };
            compute_loop<24>(read, mma);
            if (wid == 0) STAMP(12);
            f32x4* xin = reinterpret_cast<f32x4*>(shm + L_X) + wid * 320 + lane;
            f32x4* xout = reinterpret_cast<f32x4*>(shm + L_X) + (wid ^ 2) * 320 + lane;
            auto epi = [&](auto khc) {
                constexpr int KH = decltype(khc)::value;
#pragma unroll
                for (int a = 0; a < 5; ++a) xout[a * 64] = acc[a][1 - KH];
                BAR_LGKM();
#pragma unroll
                for (int a = 0; a < 5; ++a) {
                    f32x4 o = xin[a * 64];
                    f32x4 m = acc[a][KH];
                    f32x4 gg = *reinterpret_cast<const f32x4*>(tab + T_GB + 16 * a + 4 * g);
                    f32x4 y = m + o;
                    f32x2 v0 = up2(rx[a][0]) + bfr2(f32x2{gg[0], gg[1]} * bfr2(f32x2{y[0], y[1]}));
                    f32x2 v1 = up2(rx[a][1]) + bfr2(f32x2{gg[2], gg[3]} * bfr2(f32x2{y[2], y[3]}));
                    float vv[4] = {v0[0], v0[1], v1[0], v1[1]};
                    u32x2 st2 = {pk2(vv[0], vv[1]), pk2(vv[2], vv[3])};
                    *reinterpret_cast<u32x2*>(outp + (size_t)trow * D_ + n0 + 16 * a + 4 * g) = st2;
                }
            };
            if (kh) epi(IC<1>{}); else epi(IC<0>{});
            if (wid == 0) STAMP(13);
        }
    }
}

extern "C" void kernel_launch(void* const* d_in, const int* in_sizes, int n_in, void* d_out, int out_size,
                              void* d_ws, size_t ws_size, hipStream_t stream) {
    k0_prep<<<256, 256, 0, stream>>>((const u16*)d_in[0], (const u16*)d_in[6],
                                     (const u16*)d_in[7], (const u16*)d_in[8], (const u16*)d_in[9], (char*)d_ws);
    k1_chain<<<256, 512, 0, stream>>>((const u16*)d_in[0], (const u16*)d_in[1], (const u16*)d_in[2],
                                      (const u16*)d_in[3], (const u16*)d_in[4], (u16*)d_out, (char*)d_ws);
}
```

```cpp
#define OUT_NT
#include <hip/hip_runtime.h>
#include <hip/hip_bf16.h>
#include <stdint.h>

typedef __attribute__((ext_vector_type(8))) short bf16x8;
typedef __attribute__((ext_vector_type(4))) float f32x4;
typedef __attribute__((ext_vector_type(4))) unsigned u32x4;
typedef __attribute__((ext_vector_type(2))) unsigned u32x2;
typedef __attribute__((ext_vector_type(2))) __bf16 bf16x2_t;
typedef __attribute__((ext_vector_type(8))) __bf16 bf16x8_t;
typedef unsigned short u16;
typedef unsigned long long u64;

constexpr int S_ = 2048, D_ = 640, F_ = 1536;

constexpr unsigned WS_NT   = 0;
constexpr unsigned WS_PQ   = WS_NT + S_ * D_ * 2;
constexpr unsigned WS_X1   = WS_PQ + S_ * 2 * D_ * 2;
constexpr unsigned WS_HH   = WS_X1 + S_ * D_ * 2;
constexpr unsigned WS_W1U  = WS_HH + S_ * F_ * 2;
constexpr unsigned WS_W2U  = WS_W1U + D_ * F_ * 2;
constexpr unsigned WS_MOD  = WS_W2U + D_ * F_ * 2;
constexpr unsigned WS_RS   = WS_MOD + 65536;
constexpr unsigned WS_FG   = WS_RS + 2 * S_ * 4;
constexpr unsigned WS_END  = WS_FG + 3 * 32 * 32 * 8;

constexpr int L_KB_A = 0, L_KB_B = 65536;
constexpr int L_C_B = 0, L_C_A = 71680;
constexpr int L_D_A = 0, L_D_B = 81920;
constexpr int L_X = 106496;
constexpr int L_X_C = 0;
constexpr int L_MISC = 155648;
constexpr int L_TAB = 157184;
constexpr int T_CA = 0, T_SA = 80, T_GA = 160, T_GB = 240, T_CB = 320, T_SB = 960;
typedef __attribute__((ext_vector_type(4))) short s16x4;
typedef __attribute__((address_space(3))) s16x4* lds_s16x4_ptr;

__device__ __forceinline__ float bf2f(u16 v) { return __uint_as_float(((unsigned)v) << 16); }
__device__ __forceinline__ float bflo(unsigned v) { return __uint_as_float(v << 16); }
__device__ __forceinline__ float bfhi(unsigned v) { return __uint_as_float(v & 0xffff0000u); }
typedef __attribute__((ext_vector_type(2))) float f32x2;
__device__ __forceinline__ unsigned pk2v(f32x2 v) { return __builtin_bit_cast(unsigned, __builtin_convertvector(v, bf16x2_t)); }
__device__ __forceinline__ unsigned pk2(float a, float b) { return pk2v(f32x2{a, b}); }
__device__ __forceinline__ float bfr(float a) { return bflo(pk2(a, a)); }
__device__ __forceinline__ f32x2 up2(unsigned v) { return f32x2{bflo(v), bfhi(v)}; }
__device__ __forceinline__ f32x2 bfr2(f32x2 v) { return up2(pk2v(v)); }
template <int CTRL>
__device__ __forceinline__ float dpp_add(float v) {
    return v + __builtin_bit_cast(float, __builtin_amdgcn_update_dpp(0, __builtin_bit_cast(int, v), CTRL, 0xf, 0xf, true));
}
__device__ __forceinline__ float red8(float v) {
    v = dpp_add<0xB1>(v); v = dpp_add<0x4E>(v); v = dpp_add<0x141>(v);
    return v;
}
__device__ __forceinline__ float red32(float v) {
    v = red8(v); v = dpp_add<0x140>(v);
    auto r = __builtin_amdgcn_permlane16_swap(__builtin_bit_cast(unsigned, v), __builtin_bit_cast(unsigned, v), false, false);
    const unsigned r0 = r[0], r1 = r[1];
    return __uint_as_float(r0) + __uint_as_float(r1);
}

#define STAMP(idx)

#define SBAR() BAR_ONLY()
#define P0_A_SLEEP 8
#define P0_S_SLEEP 1
#define KB_RW 8
#define KB_RCB 0
#define KB_CH(st) (((st) & ~(KB_RW - 1)) | (((st) + (2 * rbl + KB_RCB * cb) * (KB_RW / 8)) & (KB_RW - 1)))
#define KB_CHA(st) KB_CH(st)
#define K0_ST(v, p) (*(p) = (v))
#define KB_B_AUX 0
#define PF_WD_J0 100
#define PF_W1_J0 100
#define B_PRE 4
#define D_PRE 2
#define POLL_SLEEP 2
#define WS_PF_NT true
#define BURST_NTE 11
#define BURST_J1 99
#define BURST_J2 99
#define BURST_WS0 0
#define BURST_WS1 0
#define BURST_NT0 0
#define BURST_NT1 0
#define BURST_JWD BURST_J2
#define KB_A_AUX 2
template <int V> struct IC { static constexpr int value = V; };
template <int N, int I = 0, class Fn>
__device__ __forceinline__ void static_for(Fn& f) {
    if constexpr (I < N) { f(IC<I>{}); static_for<N, I + 1>(f); }
}
#define WAIT_V(n) asm volatile("s_waitcnt vmcnt(%0)" ::"n"(n) : "memory")
#define BAR_ONLY() asm volatile("s_barrier" ::: "memory")
#define BAR_LGKM() do { __builtin_amdgcn_sched_barrier(0); asm volatile("" ::: "memory"); __builtin_amdgcn_s_waitcnt(0xC07F); \
    __builtin_amdgcn_s_barrier(); asm volatile("" ::: "memory"); __builtin_amdgcn_sched_barrier(0); } while (0)
#define MFMA(a, b, c) __builtin_amdgcn_mfma_f32_16x16x32_bf16(a, b, c, 0, 0, 0)
#define SGB(mask, n) __builtin_amdgcn_sched_group_barrier(mask, n, 0)

template <int AUX>
__device__ __forceinline__ void glds16(const char* g, char* l) {
    __builtin_amdgcn_global_load_lds((const unsigned*)g, (unsigned*)l, 16, 0, AUX);
}
template <bool NTP>
__device__ __forceinline__ void pf_line(const char* a, unsigned sink) {
    unsigned keep;
    if constexpr (NTP)
        asm volatile("s_mov_b32 %0, m0\n\ts_mov_b32 m0, %2\n\ts_nop 0\n\tglobal_load_lds_dword %1, off nt\n\ts_mov_b32 m0, %0"
                     : "=&s"(keep) : "v"(a), "s"(sink));
    else
        asm volatile("s_mov_b32 %0, m0\n\ts_mov_b32 m0, %2\n\ts_nop 0\n\tglobal_load_lds_dword %1, off\n\ts_mov_b32 m0, %0"
                     : "=&s"(keep) : "v"(a), "s"(sink));
}
__device__ __forceinline__ void flag_set(char* ws, unsigned off, u64 tag) {
    __hip_atomic_store(reinterpret_cast<u64*>(ws + off), tag, __ATOMIC_RELAXED, __HIP_MEMORY_SCOPE_AGENT);
}
__device__ __forceinline__ u64 flag_get(const char* ws, unsigned off) {
    return __hip_atomic_load(reinterpret_cast<const u64*>(ws + off), __ATOMIC_RELAXED, __HIP_MEMORY_SCOPE_AGENT);
}
template <int NPL>
__device__ __forceinline__ void poll_flags(const char* ws, unsigned off, int nflags, int lane, u64 tag) {
    for (int spin = 0; spin < (1 << 20); ++spin) {
        bool ok = true;
#pragma unroll
        for (int q = 0; q < NPL; ++q) {
            int i = q * 64 + lane;
            u64 v = flag_get(ws, off + (i < nflags ? i : 0) * 8);
            ok &= (v == tag);
        }
        if (__all(ok)) break;
        __builtin_amdgcn_s_sleep(POLL_SLEEP);
    }
    asm volatile("" ::: "memory");
}

template <int NT, int G, int J>
__device__ __forceinline__ void stream_tail() {
    if constexpr (J < NT) {
        constexpr int rem = (NT - 2 - J) > 0 ? (NT - 2 - J) : 0;
        WAIT_V(rem * G);
        BAR_ONLY();
        stream_tail<NT, G, J + 1>();
    }
}
struct NoHook { __device__ __forceinline__ void operator()(int) const {} };
template <int NT, int NSL, int G, int PRE = NSL, class Issue, class Hook = NoHook>
__device__ __forceinline__ void stream_run(Issue& issue, Hook hook = Hook()) {
    WAIT_V((PRE - 1) * G);
    BAR_ONLY();
#pragma nounroll
    for (int i = PRE; i < NSL; ++i) issue(i);
#pragma nounroll
    for (int j = 0; j < NT - NSL; ++j) {
        WAIT_V((NSL - 2) * G);
        BAR_ONLY();
        issue(j + NSL);
        hook(j);
    }
    stream_tail<NT, G, NT - NSL>();
}

template <int NT, class Read, class Mma, class Hook = NoHook>
__device__ __forceinline__ void compute_loop(Read& read, Mma& mma, Hook hook = Hook()) {
    BAR_LGKM();
    read(0, IC<0>{});
#pragma nounroll
    for (int j = 0; j < NT; j += 2) {
        BAR_LGKM();
        hook(j);
        read(j + 1, IC<1>{});
        mma(IC<0>{});
        BAR_LGKM();
        read(j + 2 < NT ? j + 2 : j, IC<0>{});
        mma(IC<1>{});
    }
}

__global__ __launch_bounds__(320) void k0_prep(const u16* __restrict__ x, const u16* __restrict__ wsre,
                                               const u16* __restrict__ wsim, const u16* __restrict__ w1,
                                               const u16* __restrict__ w2, const u16* __restrict__ cvec,
                                               const u16* __restrict__ wmod, char* __restrict__ ws) {
    __shared__ __attribute__((aligned(1024))) char shm[16384 + 10752 + 1024];
    const int tid = threadIdx.x, lane = tid & 63;
    const int wid = __builtin_amdgcn_readfirstlane(tid >> 6);
    const int bid = blockIdx.x;
    if (wid == 0) STAMP(20);
    if (tid < 12) reinterpret_cast<u64*>(ws + WS_FG)[bid * 12 + tid] = 0ull;
    if (wid == 4) {
        BAR_ONLY();
        return;
    }
    const int row = 2 * wid + (lane >> 5), l32 = lane & 31;
    const u16* xr = x + (size_t)(bid * 8 + row) * D_;
    u32x2 v[5];
#pragma unroll
    for (int i = 0; i < 5; ++i) v[i] = __builtin_nontemporal_load(reinterpret_cast<const u32x2*>(xr + (l32 + 32 * i) * 4));
    const int cg = bid % 60, ks = bid / 60, rg = lane >> 3, cl = lane & 7;
    const int wt = bid * 2 + (wid - 2);
    const bool isw1 = wt < 240;
    const int wl = isw1 ? wt : wt - 240;
    u32x4 w[20];
    u16 cv[3];
    if (bid < 240) {
        if (wid == 1) {
#pragma unroll
            for (int p = 0; p < 3; ++p) { int r = p * 64 + lane; cv[p] = cvec[ks * 160 + (r < 160 ? r : 0)]; }
#pragma unroll
            for (int p = 0; p < 20; ++p)
                w[p] = __builtin_nontemporal_load(reinterpret_cast<const u32x4*>(wmod + (size_t)(ks * 160 + p * 8 + rg) * 3840 + cg * 64 + cl * 8));
        } else if (wid >= 2) {
            const int N = isw1 ? F_ : D_, t = wl * 64 + lane, nbn = isw1 ? 192 : 80;
            const int kb = t / nbn, nb = t % nbn;
            const u16* src = isw1 ? w1 : w2;
#pragma unroll
            for (int k = 0; k < 8; ++k)
                w[k] = __builtin_nontemporal_load(reinterpret_cast<const u32x4*>(src + (size_t)(8 * kb + k) * N + 8 * nb));
        }
    }
    const float c_lo = bflo(__builtin_amdgcn_readlane(v[0][0], 0)), c_hi = bflo(__builtin_amdgcn_readlane(v[0][0], 32));
    const float c0 = lane < 32 ? c_lo : c_hi;
    f32x2 d[10];
    f32x2 s1 = {0.f, 0.f}, s2 = {0.f, 0.f};
#pragma unroll
    for (int i = 0; i < 5; ++i)
#pragma unroll
        for (int j = 0; j < 2; ++j) {
            f32x2 e = up2(v[i][j]) - c0;
            d[2 * i + j] = e;
            s1 += e;
            s2 += e * e;
        }
    const float m1 = red32(s1[0] + s1[1]) * (1.f / D_);
    const float m2 = red32(s2[0] + s2[1]) * (1.f / D_);
    const float rstd = 1.0f / sqrtf(fmaxf(m2 - m1 * m1, 0.f) + 1e-5f);
    const float nb_ = -m1 * rstd;
    char* sc = shm + 16384;
#pragma unroll
    for (int i = 0; i < 5; ++i) {
        u32x2 o = {pk2v(d[2 * i] * rstd + nb_), pk2v(d[2 * i + 1] * rstd + nb_)};
        *reinterpret_cast<u32x2*>(sc + row * 1344 + (l32 + 32 * i) * 8) = o;
    }
    BAR_LGKM();
    const int tq = (lane >> 2) & 3, tp = lane & 3, tg = lane >> 4;
    auto tr_store = [&](int i) {
        const char* a0 = sc + tq * 1344 + (64 * i + 16 * tg + 4 * tp) * 2;
        s16x4 lo = __builtin_amdgcn_ds_read_tr16_b64_v4i16((lds_s16x4_ptr)(a0));
        s16x4 hi = __builtin_amdgcn_ds_read_tr16_b64_v4i16((lds_s16x4_ptr)(a0 + 4 * 1344));
        u32x2 l2 = __builtin_bit_cast(u32x2, lo), h2 = __builtin_bit_cast(u32x2, hi);
        K0_ST((u32x4{l2[0], l2[1], h2[0], h2[1]}), reinterpret_cast<u32x4*>(ws + WS_NT + (bid * D_ + 64 * i + lane) * 16));
    };
    if (wid == 0) {
#pragma unroll
        for (int i = 0; i < 7; ++i) tr_store(i);
    } else {
        tr_store(6 + wid);
    }
    if (bid < 240 && wid >= 1) {
        if (wid == 1) {
            float* sl = reinterpret_cast<float*>(shm + 16384 + 10752);
#pragma unroll
            for (int p = 0; p < 3; ++p) {
                float c1 = bf2f(cv[p]);
                sl[p * 64 + lane] = bfr(c1 / (1.f + __expf(-c1)));
            }
            f32x2 a[4];
#pragma unroll
            for (int j = 0; j < 4; ++j) a[j] = f32x2{0.f, 0.f};
#pragma unroll
            for (int p = 0; p < 20; ++p) {
                const float sv = sl[p * 8 + rg];
#pragma unroll
                for (int j = 0; j < 4; ++j) a[j] += up2(w[p][j]) * sv;
            }
            float r8[8] = {a[0][0], a[0][1], a[1][0], a[1][1], a[2][0], a[2][1], a[3][0], a[3][1]};
#pragma unroll
            for (int j = 0; j < 8; ++j) {
                r8[j] += __shfl_xor(r8[j], 8);
                r8[j] += __shfl_xor(r8[j], 16);
                r8[j] += __shfl_xor(r8[j], 32);
            }
            if (lane < 16) {
                f32x4 o = lane < 8 ? f32x4{r8[0], r8[1], r8[2], r8[3]} : f32x4{r8[4], r8[5], r8[6], r8[7]};
                *reinterpret_cast<f32x4*>(ws + WS_MOD + (ks * 3840 + cg * 64 + cl * 8 + (lane >> 3) * 4) * 4) = o;
            }
        } else {
            char* sr = shm + (wid - 2) * 8192;
#pragma unroll
            for (int n = 0; n < 8; ++n) {
                u32x4 o;
#pragma unroll
                for (int p = 0; p < 4; ++p) {
                    unsigned a = w[2 * p][n >> 1], b = w[2 * p + 1][n >> 1];
                    o[p] = (n & 1) ? ((a >> 16) | (b & 0xffff0000u)) : ((a & 0xffffu) | (b << 16));
                }
                *reinterpret_cast<u32x4*>(sr + lane * 128 + ((n ^ (lane & 7)) * 16)) = o;
            }
            char* ob = ws + (isw1 ? WS_W1U : WS_W2U) + (size_t)(wl * 512) * 16;
#pragma unroll
            for (int i = 0; i < 8; ++i) {
                u32x4 o = *reinterpret_cast<const u32x4*>(sr + (8 * i + (lane >> 3)) * 128 + (((lane & 7) ^ ((lane >> 3) & 7)) * 16));
                K0_ST(o, reinterpret_cast<u32x4*>(ob + (i * 64 + lane) * 16));
            }
        }
    }
    if (wid == 0) STAMP(21);
    if (wid == 1) STAMP(23);
    if (wid == 2) STAMP(24);
}

__global__ __launch_bounds__(512, 2) void k1_chain(const u16* __restrict__ x, const u16* __restrict__ wsre,
                                                   const u16* __restrict__ wsim, const u16* __restrict__ wdre,
                                                   const u16* __restrict__ wdim, u16* __restrict__ outp,
                                                   char* __restrict__ ws) {
    __shared__ __attribute__((aligned(1024))) char shm[163840];
    const int tid = threadIdx.x, lane = tid & 63;
    const int wid = __builtin_amdgcn_readfirstlane(tid >> 6);
    const int bid = blockIdx.x;
    const int rb = (bid & 7) * 4 + (bid >> 6), cb = (bid >> 3) & 7, rbl = bid >> 6;
    const int t0 = rb * 64, n0 = cb * 80;
    const auto wrs = __builtin_amdgcn_make_buffer_rsrc(ws, 0, WS_END, 0x00020000);
    const float* tab = reinterpret_cast<const float*>(shm + L_TAB);
    constexpr u64 tag = 1ull;
    int sidx = 0;
    const int mh = wid & 1, kh = (wid >> 1) & 1, fr = lane & 15, g = lane >> 4;
    const int u = 4 * kh + g;
    const int trow = t0 + 32 * mh + 16 * kh + fr;
    u32x2 rx[5];
    const int kc_off = 5 * rbl, ke_off = 6 * rbl;
    const unsigned sink = (unsigned)(size_t)(__attribute__((address_space(3))) char*)shm + L_MISC;

    if (wid >= 5) {
        if (wid < 7) {
            const int h = wid - 5;
            const char* abase = reinterpret_cast<const char*>(h ? wsim : wsre);
            unsigned aoff[8];
#pragma unroll
            for (int q = 0; q < 8; ++q) {
                int r = 8 * q + (lane >> 3);
                aoff[q] = (unsigned)((t0 + r) * 4096 + (((lane & 7) ^ ((r >> 1) & 7)) * 16));
            }
            auto issue = [&](int st) {
                char* sl = shm + L_KB_A + (st & 3) * 16384 + h * 8192;
                const char* ag = abase + KB_CHA(st) * 128;
#pragma unroll
                for (int q = 0; q < 8; ++q) glds16<KB_A_AUX>(ag + aoff[q], sl + q * 1024);
            };
            const int l6 = lane < 60 ? lane : 59;
            const char* pfa = h == 0 ? reinterpret_cast<const char*>(cb >= 4 ? wsim : wsre) + (t0 + ((16 * cb) & 63) + (lane & 15)) * 4096 + (lane >> 4) * 128
                                     : reinterpret_cast<const char*>(wdre) + (n0 + 20 * rbl + l6 % 20) * 1280 + (l6 / 20) * 128;
            if (h == 0) {
#pragma unroll
                for (int i = 0; i < BURST_WS0; ++i) pf_line<WS_PF_NT>(pfa + i * 512, sink);
            }
#pragma nounroll
            for (int i = 0; i < 2; ++i) issue(i);
            auto hook = [&](int j) {
                if (h == 0) {
                    if (j == BURST_J1) {
#pragma unroll
                        for (int i = BURST_WS0; i < BURST_WS1; ++i) pf_line<true>(pfa + i * 512, sink);
                    } else if (j == BURST_J2) {
#pragma unroll
                        for (int i = BURST_WS1; i < 8; ++i) pf_line<true>(pfa + i * 512, sink);
                    }
                } else if (j >= PF_WD_J0 && j < PF_WD_J0 + 16 && (j & 1) == 0) {
                    const long dw = reinterpret_cast<const char*>(wdim) - reinterpret_cast<const char*>(wdre);
                    const int k = (j - PF_WD_J0) >> 1;
                    if ((k & 3) < 3 || lane < 20) pf_line<false>(pfa + (k >= 4 ? dw : 0) + (k & 3) * 384, sink);
                }
            };
            stream_run<32, 4, 8, 2>(issue, hook);
        } else {
            unsigned boff[10];
#pragma unroll
            for (int q = 0; q < 10; ++q) {
                int i = q * 64 + lane;
                boff[q] = (unsigned)(WS_NT + ((i / 80) * D_ + n0 + (i % 80)) * 16);
            }
            auto issue = [&](int st) {
                char* sl = shm + L_KB_B + (st & 3) * 10240;
                const char* bg = ws + KB_CH(st) * (8 * D_ * 16);
#pragma unroll
                for (int q = 0; q < 10; ++q) glds16<KB_B_AUX>(bg + boff[q], sl + q * 1024);
            };
            const int l6 = lane < 60 ? lane : 59, r = l6 % 20;
            const char* pfa = ws + WS_NT + ((8 * (l6 / 20) + 2 * rbl + r / 10) * D_ + n0) * 16 + (r % 10) * 128;
#pragma unroll
            for (int i = 0; i < BURST_NT0; ++i) pf_line<false>(pfa + i * (3 * 8 * D_ * 16), sink);
#pragma nounroll
            for (int i = 0; i < 2; ++i) issue(i);
            auto hook = [&](int j) {
                if (j >= PF_W1_J0 && j < PF_W1_J0 + 16 && (j & 1) == 1) {
                    int L = 64 * ((j - PF_W1_J0) >> 1) + lane;
                    L = L < 480 ? L : 479;
                    pf_line<false>(ws + WS_W1U + ((L / 6) * F_ + 192 * cb) * 16 + (6 * rbl + L % 6) * 128, sink);
                }
                if (j == BURST_J1) {
#pragma unroll
                    for (int i = BURST_NT0; i < BURST_NT1; ++i) pf_line<false>(pfa + i * (3 * 8 * D_ * 16), sink);
                } else if (j == BURST_J2) {
#pragma unroll
                    for (int i = BURST_NT1; i < BURST_NTE; ++i)
                        if (i < 10 || lane < 40) pf_line<false>(pfa + i * (3 * 8 * D_ * 16), sink);
                }
            };
            stream_run<32, 4, 10, 2>(issue, hook);
        }
        if (wid == 5) {
            unsigned aoff[8];
#pragma unroll
            for (int q = 0; q < 8; ++q) aoff[q] = (unsigned)(WS_PQ + (q * S_ + t0 + lane) * 16);
            auto issue = [&](int st) {
                char* sl = shm + L_C_A + (st & 3) * 8192;
                int ch = st + kc_off;
                ch = ch >= 20 ? ch - 20 : ch;
                const char* ag = ws + ch * (8 * S_ * 16);
#pragma unroll
                for (int q = 0; q < 8; ++q) glds16<16>(ag + aoff[q], sl + q * 1024);
            };
            BAR_ONLY();
            BAR_ONLY();
            poll_flags<1>(ws, WS_FG + (0 * 32 + rb) * 256, 32, lane, tag);
            STAMP(9);
#pragma nounroll
            for (int i = 0; i < 2; ++i) issue(i);
            stream_run<20, 4, 8, 2>(issue);
        } else {
            const int h = wid - 6;
            unsigned boff[5];
#pragma unroll
            for (int q = 0; q < 5; ++q) {
                int r = 40 * h + 8 * q + (lane >> 3);
                boff[q] = (unsigned)((n0 + r) * 1280 + (((lane & 7) ^ ((r >> 1) & 7)) * 16));
            }
            auto issue = [&](int st) {
                char* sl = shm + L_C_B + (st % 7) * 10240 + h * 5120;
                int ch = st + kc_off;
                ch = ch >= 20 ? ch - 20 : ch;
                const char* bg = ch < 10 ? reinterpret_cast<const char*>(wdre) + ch * 128
                                         : reinterpret_cast<const char*>(wdim) + (ch - 10) * 128;
#pragma unroll
                for (int q = 0; q < 5; ++q) glds16<0>(bg + boff[q], sl + q * 1024);
            };
            BAR_ONLY();
            BAR_ONLY();
#pragma nounroll
            for (int i = 0; i < B_PRE; ++i) issue(i);
            stream_run<20, 7, 5, B_PRE>(issue);
        }
    } else if (wid == 4) {
#pragma nounroll
        for (int j = 0; j < 33; ++j) SBAR();
        SBAR();
        BAR_ONLY();
#pragma nounroll
        for (int j = 0; j < 21; ++j) SBAR();
    } else {
        STAMP(0 + (wid ? 16 + wid : 0));
        {
            const float* part = reinterpret_cast<const float*>(ws + WS_MOD);
            f32x4 p[2][4];
#pragma unroll
            for (int it = 0; it < 2; ++it) {
                int qd = it * 256 + tid;
                qd = qd < 400 ? qd : 399;
                int col = qd < 20 ? 640 + n0 + qd * 4 : qd < 40 ? n0 + (qd - 20) * 4 : qd < 60 ? 1280 + n0 + (qd - 40) * 4
                        : qd < 80 ? 3200 + n0 + (qd - 60) * 4 : qd < 240 ? 2560 + (qd - 80) * 4 : 1920 + (qd - 240) * 4;
#pragma unroll
                for (int k = 0; k < 4; ++k) p[it][k] = *reinterpret_cast<const f32x4*>(part + k * 3840 + col);
            }
#pragma unroll
            for (int it = 0; it < 2; ++it) {
                int qd = it * 256 + tid;
                qd = qd < 400 ? qd : 399;
                const bool addone = qd < 20 || (qd >= 80 && qd < 240);
                f32x4 o;
#pragma unroll
                for (int r = 0; r < 4; ++r) {
                    float m = bfr((p[it][0][r] + p[it][1][r]) + (p[it][2][r] + p[it][3][r]));
                    o[r] = bfr(addone ? 1.f + m : m);
                }
                *reinterpret_cast<f32x4*>(shm + L_TAB + qd * 16) = o;
            }
        }
#pragma unroll
        for (int a = 0; a < 5; ++a)
            rx[a] = *reinterpret_cast<const u32x2*>(x + (size_t)trow * D_ + n0 + 16 * a + 4 * g);

        {
            const int moff = L_KB_A + (64 * mh + fr) * 128 + ((u ^ (fr >> 1)) * 16);
            const int noff = L_KB_B + (u * 80 + fr) * 16;
            f32x4 acc[6][4];
#pragma unroll
            for (int a = 0; a < 6; ++a)
#pragma unroll
                for (int b = 0; b < 4; ++b) acc[a][b] = f32x4{0.f, 0.f, 0.f, 0.f};
            bf16x8 mf[2][4], nf[2][5];
            const short one = 0x3F80;
            const bf16x8 ones = {one, one, one, one, one, one, one, one};
            auto read = [&](int st, auto sc_) {
                constexpr int s = decltype(sc_)::value;
                const char* al = shm + moff + (st & 3) * 16384;
                const char* bl = shm + noff + (st & 3) * 10240;
#pragma unroll
                for (int b = 0; b < 4; ++b) mf[s][b] = *reinterpret_cast<const bf16x8*>(al + b * 2048);
#pragma unroll
                for (int a = 0; a < 5; ++a) nf[s][a] = *reinterpret_cast<const bf16x8*>(bl + a * 256);
            };
            auto mma = [&](auto sc_) {
                constexpr int s = decltype(sc_)::value;
#pragma unroll
                for (int b = 0; b < 4; ++b) {
#pragma unroll
                    for (int a = 0; a < 5; ++a) acc[a][b] = MFMA(nf[s][a], mf[s][b], acc[a][b]);
                    acc[5][b] = MFMA(ones, mf[s][b], acc[5][b]);
                }
#pragma unroll
                for (int i = 0; i < 9; ++i) { SGB(0x8, 2); SGB(0x100, 1); }
                SGB(0x8, 6);
            };
            compute_loop<32>(read, mma);
            if (wid == 0) STAMP(2);
            f32x4* xin = reinterpret_cast<f32x4*>(shm + L_X) + wid * 768 + lane;
            f32x4* xout = reinterpret_cast<f32x4*>(shm + L_X) + (wid ^ 2) * 768 + lane;
            const unsigned sgn = mh ? 0x80008000u : 0u;
            auto epi = [&](auto khc) {
                constexpr int KH = decltype(khc)::value;
#pragma unroll
                for (int b2 = 0; b2 < 2; ++b2)
#pragma unroll
                    for (int a = 0; a < 6; ++a) xout[(b2 * 6 + a) * 64] = acc[a][2 * (1 - KH) + b2];
                BAR_LGKM();
#pragma unroll
                for (int b2 = 0; b2 < 2; ++b2) {
                    const float rsum = acc[5][2 * KH + b2][0] + xin[(b2 * 6 + 5) * 64][0];
                    int t = t0 + 16 * (2 * KH + b2) + fr;
#pragma unroll
                    for (int a = 0; a < 5; ++a) {
                        f32x4 o = xin[(b2 * 6 + a) * 64];
                        f32x4 m = acc[a][2 * KH + b2];
                        f32x4 cc = *reinterpret_cast<const f32x4*>(tab + T_CA + 16 * a + 4 * g);
                        f32x4 ss = *reinterpret_cast<const f32x4*>(tab + T_SA + 16 * a + 4 * g);
                        f32x4 vv = cc * (m + o) + ss * rsum;
                        int kp = 640 * mh + n0 + 16 * a + 4 * g;
                        u32x2 st2 = {pk2(vv[0], vv[1]) ^ sgn, pk2(vv[2], vv[3]) ^ sgn};
                        __builtin_amdgcn_raw_buffer_store_b64(st2, wrs, WS_PQ + ((kp >> 3) * S_ + t) * 16 + (kp & 7) * 2, 0, 16);
                    }
                }
            };
            if (kh) epi(IC<1>{}); else epi(IC<0>{});
            WAIT_V(0);
            if (lane == 0) flag_set(ws, WS_FG + (0 * 32 + rb) * 256 + (cb * 4 + wid) * 8, tag);
            if (wid == 0) STAMP(3);
            BAR_ONLY();
        }
        {
            const int moff = L_C_A + (u * 64 + 32 * mh + fr) * 16;
            const int noff = L_C_B + fr * 128 + ((u ^ (fr >> 1)) * 16);
            f32x4 acc[5][2];
#pragma unroll
            for (int a = 0; a < 5; ++a)
#pragma unroll
                for (int b = 0; b < 2; ++b) acc[a][b] = f32x4{0.f, 0.f, 0.f, 0.f};
            bf16x8 mf[2][2], nf[2][5];
            auto read = [&](int st, auto sc_) {
                constexpr int s = decltype(sc_)::value;
                const char* al = shm + moff + (st & 3) * 8192;
                const char* bl = shm + noff + (st % 7) * 10240;
#pragma unroll
                for (int b = 0; b < 2; ++b) mf[s][b] = *reinterpret_cast<const bf16x8*>(al + b * 256);
#pragma unroll
                for (int a = 0; a < 5; ++a) nf[s][a] = *reinterpret_cast<const bf16x8*>(bl + a * 2048);
            };
            auto mma = [&](auto sc_) {
                constexpr int s = decltype(sc_)::value;
#pragma unroll
                for (int b = 0; b < 2; ++b)
#pragma unroll
                    for (int a = 0; a < 5; ++a) acc[a][b] = MFMA(nf[s][a], mf[s][b], acc[a][b]);
#pragma unroll
                for (int i = 0; i < 7; ++i) { SGB(0x8, 1); SGB(0x100, 1); }
                SGB(0x8, 3);
            };
            compute_loop<20>(read, mma);
            if (wid == 0) STAMP(4);
            f32x4* xin = reinterpret_cast<f32x4*>(shm + L_X_C) + wid * 320 + lane;
            f32x4* xout = reinterpret_cast<f32x4*>(shm + L_X_C) + (wid ^ 2) * 320 + lane;
            auto epi = [&](auto khc) {
                constexpr int KH = decltype(khc)::value;
#pragma unroll
                for (int a = 0; a < 5; ++a) xout[a * 64] = acc[a][1 - KH];
                BAR_LGKM();
#pragma unroll
                for (int a = 0; a < 5; ++a) {
                    f32x4 o = xin[a * 64];
                    f32x4 m = acc[a][KH];
                    f32x4 gg = *reinterpret_cast<const f32x4*>(tab + T_GA + 16 * a + 4 * g);
                    f32x4 y = m + o;
                    f32x2 v0 = up2(rx[a][0]) + bfr2(f32x2{gg[0], gg[1]} * bfr2(f32x2{y[0], y[1]}));
                    f32x2 v1 = up2(rx[a][1]) + bfr2(f32x2{gg[2], gg[3]} * bfr2(f32x2{y[2], y[3]}));
                    float vv[4] = {v0[0], v0[1], v1[0], v1[1]};
                    rx[a] = u32x2{pk2(vv[0], vv[1]), pk2(vv[2], vv[3])};
                    __builtin_amdgcn_raw_buffer_store_b64(rx[a], wrs, WS_X1 + (trow * D_ + n0 + 16 * a + 4 * g) * 2, 0, 16);
                }
            };
            if (kh) epi(IC<1>{}); else epi(IC<0>{});
            WAIT_V(0);
            if (lane == 0) flag_set(ws, WS_FG + (1 * 32 + rb) * 256 + (cb * 4 + wid) * 8, tag);
            if (wid == 0) STAMP(5);
            BAR_ONLY();
        }
    }

    const int lw = wid - 5;
    unsigned doff[8];
#pragma unroll
    for (int q = 0; q < 8; ++q) {
        int i = (lw * 8 + q) * 64 + lane;
        doff[q] = (unsigned)(WS_W1U + ((i / 192) * F_ + cb * 192 + (i % 192)) * 16);
    }
    auto issue_d = [&](int st) {
        char* sl = shm + L_D_B + (st % 3) * 24576 + lw * 8192;
        int ch = st + (5 * rbl) / 2;
        ch = ch >= 10 ? ch - 10 : ch;
        const char* bg = ws + ch * (8 * F_ * 16);
#pragma unroll
        for (int q = 0; q < 8; ++q) glds16<0>(bg + doff[q], sl + q * 1024);
    };
    if (wid >= 4) {
        if (wid == 4) SBAR(); else BAR_ONLY();
        BAR_ONLY();
        if (wid >= 5) {
#pragma nounroll
            for (int i = 0; i < D_PRE; ++i) issue_d(i);
        }
        if (wid == 4) {
            poll_flags<1>(ws, WS_FG + (1 * 32 + rb) * 256, 32, lane, tag);
            STAMP(10);
        }
    }
    if (wid == 4) SBAR(); else BAR_ONLY();
    {
        const int row = wid * 8 + (lane >> 3), cl = lane & 7;
        u32x4 v[10];
#pragma unroll
        for (int i = 0; i < 10; ++i)
            v[i] = __builtin_amdgcn_raw_buffer_load_b128(wrs, WS_X1 + ((t0 + row) * D_ + (cl + 8 * i) * 8) * 2, 0, 16);
        f32x2 d[40];
        f32x2 s1 = {0.f, 0.f};
#pragma unroll
        for (int i = 0; i < 10; ++i)
#pragma unroll
            for (int j = 0; j < 4; ++j) { d[4 * i + j] = up2(v[i][j]); s1 += d[4 * i + j]; }
        const float mean = red8(s1[0] + s1[1]) * (1.f / D_);
        f32x2 s2 = {0.f, 0.f};
#pragma unroll
        for (int i = 0; i < 40; ++i) { d[i] -= mean; s2 += d[i] * d[i]; }
        const float rstd = 1.0f / sqrtf(red8(s2[0] + s2[1]) * (1.f / D_) + 1e-5f);
#pragma unroll
        for (int i = 0; i < 10; ++i) {
            const int un = cl + 8 * i;
            f32x4 c0 = *reinterpret_cast<const f32x4*>(tab + T_CB + un * 8), c1 = *reinterpret_cast<const f32x4*>(tab + T_CB + un * 8 + 4);
            f32x4 t0_ = *reinterpret_cast<const f32x4*>(tab + T_SB + un * 8), t1_ = *reinterpret_cast<const f32x4*>(tab + T_SB + un * 8 + 4);
            u32x4 o;
            o[0] = pk2v(d[4 * i + 0] * (f32x2{c0[0], c0[1]} * rstd) + f32x2{t0_[0], t0_[1]});
            o[1] = pk2v(d[4 * i + 1] * (f32x2{c0[2], c0[3]} * rstd) + f32x2{t0_[2], t0_[3]});
            o[2] = pk2v(d[4 * i + 2] * (f32x2{c1[0], c1[1]} * rstd) + f32x2{t1_[0], t1_[1]});
            o[3] = pk2v(d[4 * i + 3] * (f32x2{c1[2], c1[3]} * rstd) + f32x2{t1_[2], t1_[3]});
            *reinterpret_cast<u32x4*>(shm + L_D_A + un * 1024 + ((row ^ (un & 7)) * 16)) = o;
        }
        asm volatile("s_waitcnt vmcnt(0) lgkmcnt(0)" ::: "memory");
        if (wid == 0) STAMP(30);
        if (wid == 7) STAMP(31);
    }

    if (wid >= 5) {
        stream_run<10, 3, 8, D_PRE>(issue_d);
        if (wid == 5) {
            unsigned aoff[8];
#pragma unroll
            for (int q = 0; q < 8; ++q) aoff[q] = (unsigned)(WS_HH + (q * S_ + t0 + lane) * 16);
            auto issue = [&](int st) {
                char* sl = shm + L_C_A + (st & 3) * 8192;
                int ch = st + ke_off;
                ch = ch >= 24 ? ch - 24 : ch;
                const char* ag = ws + ch * (8 * S_ * 16);
#pragma unroll
                for (int q = 0; q < 8; ++q) glds16<16>(ag + aoff[q], sl + q * 1024);
            };
            BAR_ONLY();
            BAR_ONLY();
            poll_flags<1>(ws, WS_FG + (2 * 32 + rb) * 256, 32, lane, tag);
            STAMP(11);
#pragma nounroll
            for (int i = 0; i < 2; ++i) issue(i);
            stream_run<24, 4, 8, 2>(issue);
        } else {
            const int h = wid - 6;
            unsigned boff[5];
#pragma unroll
            for (int q = 0; q < 5; ++q) {
                int i = (h * 5 + q) * 64 + lane;
                boff[q] = (unsigned)(WS_W2U + ((i / 80) * D_ + n0 + (i % 80)) * 16);
            }
            auto issue = [&](int st) {
                char* sl = shm + L_C_B + (st % 7) * 10240 + h * 5120;
                int ch = st + ke_off;
                ch = ch >= 24 ? ch - 24 : ch;
                const char* bg = ws + ch * (8 * D_ * 16);
#pragma unroll
                for (int q = 0; q < 5; ++q) glds16<0>(bg + boff[q], sl + q * 1024);
            };
            BAR_ONLY();
            BAR_ONLY();
#pragma nounroll
            for (int i = 0; i < B_PRE; ++i) issue(i);
            stream_run<24, 7, 5, B_PRE>(issue);
        }
    } else if (wid == 4) {
#pragma nounroll
        for (int j = 0; j < 12; ++j) SBAR();
    } else {
        {
            const int nh = mh;
            const int moff = L_D_A + u * 1024 + ((fr ^ u) * 16);
            const int noff = L_D_B + (u * 192 + 96 * nh + fr) * 16;
            f32x4 acc[6][4];
#pragma unroll
            for (int a = 0; a < 6; ++a)
#pragma unroll
                for (int b = 0; b < 4; ++b) acc[a][b] = f32x4{0.f, 0.f, 0.f, 0.f};
            bf16x8 mf[2][4], nf[2][6];
            auto read = [&](int st, auto sc_) {
                constexpr int s = decltype(sc_)::value;
                int ch = st + (5 * rbl) / 2;
                ch = ch >= 10 ? ch - 10 : ch;
                const char* al = shm + moff + ch * 8192;
                const char* bl = shm + noff + (st % 3) * 24576;
#pragma unroll
                for (int b = 0; b < 4; ++b) mf[s][b] = *reinterpret_cast<const bf16x8*>(al + b * 256);
#pragma unroll
                for (int a = 0; a < 6; ++a) nf[s][a] = *reinterpret_cast<const bf16x8*>(bl + a * 256);
            };
            auto mma = [&](auto sc_) {
                constexpr int s = decltype(sc_)::value;
#pragma unroll
                for (int b = 0; b < 4; ++b)
#pragma unroll
                    for (int a = 0; a < 6; ++a) acc[a][b] = MFMA(nf[s][a], mf[s][b], acc[a][b]);
#pragma unroll
                for (int i = 0; i < 10; ++i) { SGB(0x8, 2); SGB(0x100, 1); }
                SGB(0x8, 4);
            };
            compute_loop<10>(read, mma);
            if (wid == 0) STAMP(6);
            f32x4* xin = reinterpret_cast<f32x4*>(shm + L_X) + wid * 768 + lane;
            f32x4* xout = reinterpret_cast<f32x4*>(shm + L_X) + (wid ^ 2) * 768 + lane;
            auto epi = [&](auto khc) {
                constexpr int KH = decltype(khc)::value;
#pragma unroll
                for (int b2 = 0; b2 < 2; ++b2)
#pragma unroll
                    for (int a = 0; a < 6; ++a) xout[(b2 * 6 + a) * 64] = acc[a][2 * (1 - KH) + b2];
                BAR_LGKM();
#pragma unroll
                for (int b2 = 0; b2 < 2; ++b2) {
                    int t = t0 + 16 * (2 * KH + b2) + fr;
#pragma unroll
                    for (int a = 0; a < 6; ++a) {
                        f32x4 o = xin[(b2 * 6 + a) * 64];
                        f32x4 m = acc[a][2 * KH + b2];
                        float vv[4];
#pragma unroll
                        for (int r = 0; r < 4; ++r) vv[r] = fmaxf(m[r] + o[r], 0.f);
                        int f = cb * 192 + 96 * nh + 16 * a + 4 * g;
                        u32x2 st2 = {pk2(vv[0], vv[1]), pk2(vv[2], vv[3])};
                        __builtin_amdgcn_raw_buffer_store_b64(st2, wrs, WS_HH + ((f >> 3) * S_ + t) * 16 + (f & 7) * 2, 0, 16);
                    }
                }
            };
            if (kh) epi(IC<1>{}); else epi(IC<0>{});
            WAIT_V(0);
            if (lane == 0) flag_set(ws, WS_FG + (2 * 32 + rb) * 256 + (cb * 4 + wid) * 8, tag);
            if (wid == 0) STAMP(7);
            BAR_ONLY();
        }
        {
            const int moff = L_C_A + (u * 64 + 32 * mh + fr) * 16;
            const int noff = L_C_B + (u * 80 + fr) * 16;
            f32x4 acc[5][2];
#pragma unroll
            for (int a = 0; a < 5; ++a)
#pragma unroll
                for (int b = 0; b < 2; ++b) acc[a][b] = f32x4{0.f, 0.f, 0.f, 0.f};
            bf16x8 mf[2][2], nf[2][5];
            auto read = [&](int st, auto sc_) {
                constexpr int s = decltype(sc_)::value;
                const char* al = shm + moff + (st & 3) * 8192;
                const char* bl = shm + noff + (st % 7) * 10240;
#pragma unroll
                for (int b = 0; b < 2; ++b) mf[s][b] = *reinterpret_cast<const bf16x8*>(al + b * 256);
#pragma unroll
                for (int a = 0; a < 5; ++a) nf[s][a] = *reinterpret_cast<const bf16x8*>(bl + a * 256);
            };
            auto mma = [&](auto sc_) {
                constexpr int s = decltype(sc_)::value;
#pragma unroll
                for (int b = 0; b < 2; ++b)
#pragma unroll
                    for (int a = 0; a < 5; ++a) acc[a][b] = MFMA(nf[s][a], mf[s][b], acc[a][b]);
#pragma unroll
                for (int i = 0; i < 7; ++i) { SGB(0x8, 1); SGB(0x100, 1); }
                SGB(0x8, 3);
            };
            compute_loop<24>(read, mma);
            if (wid == 0) STAMP(12);
            f32x4* xin = reinterpret_cast<f32x4*>(shm + L_X) + wid * 320 + lane;
            f32x4* xout = reinterpret_cast<f32x4*>(shm + L_X) + (wid ^ 2) * 320 + lane;
            auto epi = [&](auto khc) {
                constexpr int KH = decltype(khc)::value;
#pragma unroll
                for (int a = 0; a < 5; ++a) xout[a * 64] = acc[a][1 - KH];
                BAR_LGKM();
#pragma unroll
                for (int a = 0; a < 5; ++a) {
                    f32x4 o = xin[a * 64];
                    f32x4 m = acc[a][KH];
                    f32x4 gg = *reinterpret_cast<const f32x4*>(tab + T_GB + 16 * a + 4 * g);
                    f32x4 y = m + o;
                    f32x2 v0 = up2(rx[a][0]) + bfr2(f32x2{gg[0], gg[1]} * bfr2(f32x2{y[0], y[1]}));
                    f32x2 v1 = up2(rx[a][1]) + bfr2(f32x2{gg[2], gg[3]} * bfr2(f32x2{y[2], y[3]}));
                    float vv[4] = {v0[0], v0[1], v1[0], v1[1]};
                    u32x2 st2 = {pk2(vv[0], vv[1]), pk2(vv[2], vv[3])};
                    __builtin_nontemporal_store(st2, reinterpret_cast<u32x2*>(outp + (size_t)trow * D_ + n0 + 16 * a + 4 * g));
                }
            };
            if (kh) epi(IC<1>{}); else epi(IC<0>{});
            if (wid == 0) STAMP(13);
        }
    }
}

extern "C" void kernel_launch(void* const* d_in, const int* in_sizes, int n_in, void* d_out, int out_size,
                              void* d_ws, size_t ws_size, hipStream_t stream) {
    k0_prep<<<256, 320, 0, stream>>>((const u16*)d_in[0], (const u16*)d_in[1], (const u16*)d_in[2], (const u16*)d_in[6],
                                     (const u16*)d_in[7], (const u16*)d_in[8], (const u16*)d_in[9], (char*)d_ws);
    k1_chain<<<256, 512, 0, stream>>>((const u16*)d_in[0], (const u16*)d_in[1], (const u16*)d_in[2],
                                      (const u16*)d_in[3], (const u16*)d_in[4], (u16*)d_out, (char*)d_ws);
}
```

```cpp
#include <hip/hip_runtime.h>
#include <hip/hip_bf16.h>
#include <stdint.h>

typedef __attribute__((ext_vector_type(8))) short bf16x8;
typedef __attribute__((ext_vector_type(4))) float f32x4;
typedef __attribute__((ext_vector_type(4))) unsigned u32x4;
typedef __attribute__((ext_vector_type(2))) unsigned u32x2;
typedef __attribute__((ext_vector_type(2))) __bf16 bf16x2_t;
typedef unsigned short u16;
typedef unsigned long long u64;

constexpr int S_ = 2048, D_ = 640, F_ = 1536;

constexpr unsigned WS_NT   = 0;
constexpr unsigned WS_PQ   = WS_NT + S_ * D_ * 2;
constexpr unsigned WS_X1   = WS_PQ + S_ * 2 * D_ * 2;
constexpr unsigned WS_HH   = WS_X1 + S_ * D_ * 2;
constexpr unsigned WS_W1U  = WS_HH + S_ * F_ * 2;
constexpr unsigned WS_W2U  = WS_W1U + D_ * F_ * 2;
constexpr unsigned WS_MOD  = WS_W2U + D_ * F_ * 2;
constexpr unsigned WS_ST   = WS_MOD + 65536;
constexpr unsigned WS_FG   = WS_ST + S_ * 8 * 8;
constexpr unsigned WS_END  = WS_FG + 3 * 32 * 32 * 8;

constexpr int L_KB_A = 0, L_KB_B = 65536;
constexpr int L_C_B = 0, L_C_A = 61440;
constexpr int L_D_B = 0, L_D_A = 73728;
constexpr int L_X = 106496;
constexpr int L_MISC = 155648;
constexpr int L_TAB = 157184;
constexpr int T_CA = 0, T_SA = 80, T_GA = 160, T_GB = 240, T_CB = 320;
constexpr int L_XR = L_TAB + 1600;
constexpr int L_RS = L_XR + 2576;
typedef __attribute__((ext_vector_type(4))) short s16x4;
typedef __attribute__((address_space(3))) s16x4* lds_s16x4_ptr;

__device__ __forceinline__ float bf2f(u16 v) { return __uint_as_float(((unsigned)v) << 16); }
__device__ __forceinline__ float bflo(unsigned v) { return __uint_as_float(v << 16); }
__device__ __forceinline__ float bfhi(unsigned v) { return __uint_as_float(v & 0xffff0000u); }
typedef __attribute__((ext_vector_type(2))) float f32x2;
__device__ __forceinline__ unsigned pk2v(f32x2 v) { return __builtin_bit_cast(unsigned, __builtin_convertvector(v, bf16x2_t)); }
__device__ __forceinline__ unsigned pk2(float a, float b) { return pk2v(f32x2{a, b}); }
__device__ __forceinline__ float bfr(float a) { return bflo(pk2(a, a)); }
__device__ __forceinline__ f32x2 up2(unsigned v) { return f32x2{bflo(v), bfhi(v)}; }
__device__ __forceinline__ f32x2 bfr2(f32x2 v) { return up2(pk2v(v)); }
__device__ __forceinline__ float red_g(float v) {
    auto r = __builtin_amdgcn_permlane16_swap(__float_as_uint(v), __float_as_uint(v), false, false);
    const unsigned r0 = r[0], r1 = r[1];
    v = __uint_as_float(r0) + __uint_as_float(r1);
    auto q = __builtin_amdgcn_permlane32_swap(__float_as_uint(v), __float_as_uint(v), false, false);
    const unsigned q0 = q[0], q1 = q[1];
    return __uint_as_float(q0) + __uint_as_float(q1);
}
template <int CTRL>
__device__ __forceinline__ float dpp_add(float v) {
    return v + __builtin_bit_cast(float, __builtin_amdgcn_update_dpp(0, __builtin_bit_cast(int, v), CTRL, 0xf, 0xf, true));
}
__device__ __forceinline__ float red8(float v) {
    v = dpp_add<0xB1>(v); v = dpp_add<0x4E>(v); v = dpp_add<0x141>(v);
    return v;
}
__device__ __forceinline__ float red32(float v) {
    v = red8(v); v = dpp_add<0x140>(v);
    auto r = __builtin_amdgcn_permlane16_swap(__builtin_bit_cast(unsigned, v), __builtin_bit_cast(unsigned, v), false, false);
    const unsigned r0 = r[0], r1 = r[1];
    return __uint_as_float(r0) + __uint_as_float(r1);
}

constexpr int K0_PFN = 3, K1_PFN = 3;
constexpr int B_PRE2 = 2, D_PRE = 2;
template <int V> struct IC { static constexpr int value = V; };
#define WAIT_V(n) asm volatile("s_waitcnt vmcnt(%0)" ::"n"(n) : "memory")
#define BAR_ONLY() asm volatile("s_barrier" ::: "memory")
#define BAR_LGKM() do { __builtin_amdgcn_sched_barrier(0); asm volatile("" ::: "memory"); __builtin_amdgcn_s_waitcnt(0xC07F); \
    __builtin_amdgcn_s_barrier(); asm volatile("" ::: "memory"); __builtin_amdgcn_sched_barrier(0); } while (0)
#define MFMA(a, b, c) __builtin_amdgcn_mfma_f32_16x16x32_bf16(a, b, c, 0, 0, 0)
#define SGB(mask, n) __builtin_amdgcn_sched_group_barrier(mask, n, 0)

template <int AUX>
__device__ __forceinline__ void glds16(const char* g, char* l) {
    __builtin_amdgcn_global_load_lds((const unsigned*)g, (unsigned*)l, 16, 0, AUX);
}
__device__ __forceinline__ void pf_line(const char* a, unsigned sink) {
    unsigned keep;
    asm volatile("s_mov_b32 %0, m0\n\ts_mov_b32 m0, %2\n\ts_nop 0\n\tglobal_load_lds_dword %1, off nt\n\ts_mov_b32 m0, %0"
                 : "=&s"(keep) : "v"(a), "s"(sink));
}
__device__ __forceinline__ void flag_set(char* ws, unsigned off, u64 tag) {
    __hip_atomic_store(reinterpret_cast<u64*>(ws + off), tag, __ATOMIC_RELAXED, __HIP_MEMORY_SCOPE_AGENT);
}
__device__ __forceinline__ u64 flag_get(const char* ws, unsigned off) {
    return __hip_atomic_load(reinterpret_cast<const u64*>(ws + off), __ATOMIC_RELAXED, __HIP_MEMORY_SCOPE_AGENT);
}
template <int NPL>
__device__ __forceinline__ void poll_flags(const char* ws, unsigned off, int nflags, int lane, u64 tag) {
    for (int spin = 0; spin < (1 << 20); ++spin) {
        bool ok = true;
#pragma unroll
        for (int q = 0; q < NPL; ++q) {
            int i = q * 64 + lane;
            u64 v = flag_get(ws, off + (i < nflags ? i : 0) * 8);
            ok &= (v == tag);
        }
        if (__all(ok)) break;
        __builtin_amdgcn_s_sleep(2);
    }
    asm volatile("" ::: "memory");
}

template <int NT, int G, int J>
__device__ __forceinline__ void stream_tail() {
    if constexpr (J < NT) {
        constexpr int rem = (NT - 2 - J) > 0 ? (NT - 2 - J) : 0;
        WAIT_V(rem * G);
        BAR_ONLY();
        stream_tail<NT, G, J + 1>();
    }
}
template <int NT, int NSL, int G, int PRE, class Issue>
__device__ __forceinline__ void stream_run(Issue& issue) {
    WAIT_V((PRE - 1) * G);
    BAR_ONLY();
#pragma nounroll
    for (int i = PRE; i < NSL; ++i) issue(i);
#pragma nounroll
    for (int j = 0; j < NT - NSL; ++j) {
        WAIT_V((NSL - 2) * G);
        BAR_ONLY();
        issue(j + NSL);
    }
    stream_tail<NT, G, NT - NSL>();
}

template <int NT, class Read, class Mma>
__device__ __forceinline__ void compute_loop(Read& read, Mma& mma) {
    BAR_LGKM();
    read(0, IC<0>{});
#pragma nounroll
    for (int j = 0; j < NT; j += 2) {
        BAR_LGKM();
        read(j + 1, IC<1>{});
        mma(IC<0>{});
        BAR_LGKM();
        read(j + 2 < NT ? j + 2 : j, IC<0>{});
        mma(IC<1>{});
    }
}

__global__ __launch_bounds__(576) void k0_prep(const u16* __restrict__ x, const u16* __restrict__ wsre,
                                               const u16* __restrict__ wsim, const u16* __restrict__ w1,
                                               const u16* __restrict__ w2, const u16* __restrict__ cvec,
                                               const u16* __restrict__ wmod, char* __restrict__ ws) {
    __shared__ __attribute__((aligned(1024))) char shm[16384 + 10752 + 2048];
    const int tid = threadIdx.x, lane = tid & 63;
    const int wid = __builtin_amdgcn_readfirstlane(tid >> 6);
    const int bid = blockIdx.x;
    if (wid == 8) {
        const int rb = (bid & 7) * 4 + (bid >> 6), cb = (bid >> 3) & 7;
        const unsigned sink = (unsigned)(size_t)(__attribute__((address_space(3))) char*)shm + 16384 + 10752 + 1536;
        const char* pfa = reinterpret_cast<const char*>(cb >= 4 ? wsim : wsre) + (rb * 64 + ((16 * cb) & 63) + (lane & 15)) * 4096 + (lane >> 4) * 128;
#pragma unroll
        for (int i = 0; i < K0_PFN; ++i) pf_line(pfa + i * 512, sink);
        BAR_ONLY();
        WAIT_V(0);
        return;
    }
    if (wid >= 6) {
        if (bid >= 240) return;
        const int wt = bid * 2 + (wid - 6);
        const bool isw1 = wt < 240;
        const int wl = isw1 ? wt : wt - 240;
        const int N = isw1 ? F_ : D_, t = wl * 64 + lane, nbn = isw1 ? 192 : 80;
        const int kb = t / nbn, nb = t % nbn;
        const u16* src = isw1 ? w1 : w2;
        u32x4 w[8];
#pragma unroll
        for (int k = 0; k < 8; ++k)
            w[k] = __builtin_nontemporal_load(reinterpret_cast<const u32x4*>(src + (size_t)(8 * kb + k) * N + 8 * nb));
        BAR_ONLY();
        char* sr = shm + (wid - 6) * 8192;
#pragma unroll
        for (int n = 0; n < 8; ++n) {
            u32x4 o;
#pragma unroll
            for (int p = 0; p < 4; ++p) {
                unsigned a = w[2 * p][n >> 1], b = w[2 * p + 1][n >> 1];
                o[p] = (n & 1) ? ((a >> 16) | (b & 0xffff0000u)) : ((a & 0xffffu) | (b << 16));
            }
            *reinterpret_cast<u32x4*>(sr + lane * 128 + ((n ^ (lane & 7)) * 16)) = o;
        }
        char* ob = ws + (isw1 ? WS_W1U : WS_W2U) + (size_t)(wl * 512) * 16;
#pragma unroll
        for (int i = 0; i < 8; ++i) {
            u32x4 o = *reinterpret_cast<const u32x4*>(sr + (8 * i + (lane >> 3)) * 128 + (((lane & 7) ^ ((lane >> 3) & 7)) * 16));
            *reinterpret_cast<u32x4*>(ob + (i * 64 + lane) * 16) = o;
        }
        return;
    }
    if (wid >= 4) {
        if (bid >= 240) return;
        const int hw = wid - 4, cg = bid % 60, ks = bid / 60, rg = lane >> 3, cl = lane & 7;
        const int r0 = ks * 160 + hw * 80;
        u16 cv[2];
#pragma unroll
        for (int p = 0; p < 2; ++p) { int r = p * 64 + lane; cv[p] = cvec[r0 + (r < 80 ? r : 0)]; }
        u32x4 w[10];
#pragma unroll
        for (int p = 0; p < 10; ++p)
            w[p] = __builtin_nontemporal_load(reinterpret_cast<const u32x4*>(wmod + (size_t)(r0 + p * 8 + rg) * 3840 + cg * 64 + cl * 8));
        BAR_ONLY();
        float* sl = reinterpret_cast<float*>(shm + 16384 + 10752) + hw * 128;
#pragma unroll
        for (int p = 0; p < 2; ++p) {
            float c1 = bf2f(cv[p]);
            sl[p * 64 + lane] = bfr(c1 / (1.f + __expf(-c1)));
        }
        f32x2 a[4];
#pragma unroll
        for (int j = 0; j < 4; ++j) a[j] = f32x2{0.f, 0.f};
#pragma unroll
        for (int p = 0; p < 10; ++p) {
            const float sv = sl[p * 8 + rg];
#pragma unroll
            for (int j = 0; j < 4; ++j) a[j] += up2(w[p][j]) * sv;
        }
        float r8[8] = {a[0][0], a[0][1], a[1][0], a[1][1], a[2][0], a[2][1], a[3][0], a[3][1]};
#pragma unroll
        for (int j = 0; j < 8; ++j) r8[j] = red_g(dpp_add<0x128>(r8[j]));
        f32x4* cmb = reinterpret_cast<f32x4*>(shm + 16384 + 10752 + 1024);
        const f32x4 o = lane < 8 ? f32x4{r8[0], r8[1], r8[2], r8[3]} : f32x4{r8[4], r8[5], r8[6], r8[7]};
        if (hw == 1 && lane < 16) cmb[lane] = o;
        BAR_LGKM();
        if (hw == 0 && lane < 16)
            *reinterpret_cast<f32x4*>(ws + WS_MOD + (ks * 3840 + cg * 64 + cl * 8 + (lane >> 3) * 4) * 4) = o + cmb[lane];
        return;
    }
    if (tid < 12) reinterpret_cast<u64*>(ws + WS_FG)[bid * 12 + tid] = 0ull;
    const int row = 2 * wid + (lane >> 5), l32 = lane & 31;
    const u16* xr = x + (size_t)(bid * 8 + row) * D_;
    u32x2 v[5];
#pragma unroll
    for (int i = 0; i < 5; ++i) v[i] = __builtin_nontemporal_load(reinterpret_cast<const u32x2*>(xr + (l32 + 32 * i) * 4));
    const float c_lo = bflo(__builtin_amdgcn_readlane(v[0][0], 0)), c_hi = bflo(__builtin_amdgcn_readlane(v[0][0], 32));
    const float c0 = lane < 32 ? c_lo : c_hi;
    f32x2 d[10];
    f32x2 s1 = {0.f, 0.f}, s2 = {0.f, 0.f};
#pragma unroll
    for (int i = 0; i < 5; ++i)
#pragma unroll
        for (int j = 0; j < 2; ++j) {
            f32x2 e = up2(v[i][j]) - c0;
            d[2 * i + j] = e;
            s1 += e;
            s2 += e * e;
        }
    const float m1 = red32(s1[0] + s1[1]) * (1.f / D_);
    const float m2 = red32(s2[0] + s2[1]) * (1.f / D_);
    const float rstd = 1.0f / sqrtf(fmaxf(m2 - m1 * m1, 0.f) + 1e-5f);
    const float nb_ = -m1 * rstd;
    char* sc = shm + 16384;
#pragma unroll
    for (int i = 0; i < 5; ++i) {
        u32x2 o = {pk2v(d[2 * i] * rstd + nb_), pk2v(d[2 * i + 1] * rstd + nb_)};
        *reinterpret_cast<u32x2*>(sc + row * 1344 + (l32 + 32 * i) * 8) = o;
    }
    BAR_LGKM();
    const int tq = (lane >> 2) & 3, tp = lane & 3, tg = lane >> 4;
#pragma unroll
    for (int k = 0; k < 3; ++k) {
        const int i = wid + 4 * k;
        if (i < 10) {
            const char* a0 = sc + tq * 1344 + (64 * i + 16 * tg + 4 * tp) * 2;
            s16x4 lo = __builtin_amdgcn_ds_read_tr16_b64_v4i16((lds_s16x4_ptr)(a0));
            s16x4 hi = __builtin_amdgcn_ds_read_tr16_b64_v4i16((lds_s16x4_ptr)(a0 + 4 * 1344));
            u32x2 l2 = __builtin_bit_cast(u32x2, lo), h2 = __builtin_bit_cast(u32x2, hi);
            *reinterpret_cast<u32x4*>(ws + WS_NT + (bid * D_ + 64 * i + lane) * 16) = u32x4{l2[0], l2[1], h2[0], h2[1]};
        }
    }
}

__global__ __launch_bounds__(512, 2) void k1_chain(const u16* __restrict__ x, const u16* __restrict__ wsre,
                                                   const u16* __restrict__ wsim, const u16* __restrict__ wdre,
                                                   const u16* __restrict__ wdim, u16* __restrict__ outp,
                                                   char* __restrict__ ws) {
    __shared__ __attribute__((aligned(1024))) char shm[163840];
    const int tid = threadIdx.x, lane = tid & 63;
    const int wid = __builtin_amdgcn_readfirstlane(tid >> 6);
    const int bid = blockIdx.x;
    const int rb = (bid & 7) * 4 + (bid >> 6), cb = (bid >> 3) & 7, rbl = bid >> 6;
    const int t0 = rb * 64, n0 = cb * 80;
    const auto wrs = __builtin_amdgcn_make_buffer_rsrc(ws, 0, WS_END, 0x00020000);
    const float* tab = reinterpret_cast<const float*>(shm + L_TAB);
    constexpr u64 tag = 1ull;
    const int mh = wid & 1, kh = (wid >> 1) & 1, fr = lane & 15, g = lane >> 4;
    const int u = 4 * kh + g;
    const int trow = t0 + 32 * mh + 16 * kh + fr;
    u32x2 rx[5];
    const int kc_off = 5 * rbl, ke_off = 6 * rbl;
    auto kb_chunk = [&](int st) { return (st & ~7) | ((st + 2 * rbl) & 7); };
    const unsigned sink = (unsigned)(size_t)(__attribute__((address_space(3))) char*)shm + L_MISC;

    if (wid >= 5) {
        if (wid < 7) {
            const int h = wid - 5;
            const char* abase = reinterpret_cast<const char*>(h ? wsim : wsre);
            unsigned aoff[8];
#pragma unroll
            for (int q = 0; q < 8; ++q) {
                int r = 8 * q + (lane >> 3);
                aoff[q] = (unsigned)((t0 + r) * 4096 + (((lane & 7) ^ ((r >> 1) & 7)) * 16));
            }
            auto issue = [&](int st) {
                char* sl = shm + L_KB_A + (st & 3) * 16384 + h * 8192;
                const char* ag = abase + kb_chunk(st) * 128;
#pragma unroll
                for (int q = 0; q < 8; ++q) glds16<2>(ag + aoff[q], sl + q * 1024);
            };
            if (h == 0) {
                const char* pfa = reinterpret_cast<const char*>(cb >= 4 ? wsim : wsre) + (t0 + ((16 * cb) & 63) + (lane & 15)) * 4096 + (lane >> 4) * 128;
#pragma unroll
                for (int i = K0_PFN; i < K0_PFN + K1_PFN; ++i) pf_line(pfa + i * 512, sink);
            }
#pragma nounroll
            for (int i = 0; i < 2; ++i) issue(i);
            stream_run<32, 4, 8, 2>(issue);
        } else {
            unsigned boff[10];
#pragma unroll
            for (int q = 0; q < 10; ++q) {
                int i = q * 64 + lane;
                boff[q] = (unsigned)(WS_NT + ((i / 80) * D_ + n0 + (i % 80)) * 16);
            }
            auto issue = [&](int st) {
                char* sl = shm + L_KB_B + (st & 3) * 10240;
                const char* bg = ws + kb_chunk(st) * (8 * D_ * 16);
#pragma unroll
                for (int q = 0; q < 10; ++q) glds16<0>(bg + boff[q], sl + q * 1024);
            };
#pragma nounroll
            for (int i = 0; i < 2; ++i) issue(i);
            stream_run<32, 4, 10, 2>(issue);
        }
        if (wid == 5) {
            unsigned aoff[8];
#pragma unroll
            for (int q = 0; q < 8; ++q) aoff[q] = (unsigned)(WS_PQ + (q * S_ + t0 + lane) * 16);
            auto issue = [&](int st) {
                char* sl = shm + L_C_A + (st % 6) * 8192;
                int ch = st + kc_off;
                ch = ch >= 20 ? ch - 20 : ch;
                const char* ag = ws + ch * (8 * S_ * 16);
#pragma unroll
                for (int q = 0; q < 8; ++q) glds16<16>(ag + aoff[q], sl + q * 1024);
            };
            BAR_ONLY();
            BAR_ONLY();
            poll_flags<1>(ws, WS_FG + (0 * 32 + rb) * 256, 32, lane, tag);
            auto issue2 = [&](int S) { issue(2 * S); issue(2 * S + 1); };
            issue2(0);
            stream_run<10, 3, 16, 1>(issue2);
        } else {
            const int h = wid - 6;
            unsigned boff[5];
#pragma unroll
            for (int q = 0; q < 5; ++q) {
                int r = 40 * h + 8 * q + (lane >> 3);
                boff[q] = (unsigned)((n0 + r) * 1280 + (((lane & 7) ^ ((r >> 1) & 7)) * 16));
            }
            auto issue = [&](int st) {
                char* sl = shm + L_C_B + (st % 6) * 10240 + h * 5120;
                int ch = st + kc_off;
                ch = ch >= 20 ? ch - 20 : ch;
                const char* bg = ch < 10 ? reinterpret_cast<const char*>(wdre) + ch * 128
                                         : reinterpret_cast<const char*>(wdim) + (ch - 10) * 128;
#pragma unroll
                for (int q = 0; q < 5; ++q) glds16<0>(bg + boff[q], sl + q * 1024);
            };
            BAR_ONLY();
            BAR_ONLY();
            auto issue2 = [&](int S) { issue(2 * S); issue(2 * S + 1); };
#pragma nounroll
            for (int i = 0; i < B_PRE2; ++i) issue2(i);
            stream_run<10, 3, 10, B_PRE2>(issue2);
        }
    } else if (wid == 4) {
#pragma nounroll
        for (int j = 0; j < 33; ++j) BAR_ONLY();
        BAR_ONLY();
        BAR_ONLY();
#pragma nounroll
        for (int j = 0; j < 11; ++j) BAR_ONLY();
    } else {
        {
            const float* part = reinterpret_cast<const float*>(ws + WS_MOD);
            f32x4 p[2][4];
#pragma unroll
            for (int it = 0; it < 2; ++it) {
                int qd = it * 256 + tid;
                qd = qd < 420 ? qd : 419;
                int col = qd < 20 ? 640 + n0 + qd * 4 : qd < 40 ? n0 + (qd - 20) * 4 : qd < 60 ? 1280 + n0 + (qd - 40) * 4
                        : qd < 80 ? 3200 + n0 + (qd - 60) * 4 : qd < 100 ? 2560 + n0 + (qd - 80) * 4
                        : qd < 260 ? 2560 + (qd - 100) * 4 : 1920 + (qd - 260) * 4;
#pragma unroll
                for (int k = 0; k < 4; ++k) p[it][k] = *reinterpret_cast<const f32x4*>(part + k * 3840 + col);
            }
#pragma unroll
            for (int it = 0; it < 2; ++it) {
                int qd = it * 256 + tid;
                qd = qd < 420 ? qd : 419;
                const bool addone = qd < 20 || (qd >= 80 && qd < 260);
                f32x4 o;
#pragma unroll
                for (int r = 0; r < 4; ++r) {
                    float m = bfr((p[it][0][r] + p[it][1][r]) + (p[it][2][r] + p[it][3][r]));
                    o[r] = bfr(addone ? 1.f + m : m);
                }
                if (qd < 100) {
                    *reinterpret_cast<f32x4*>(shm + L_TAB + qd * 16) = o;
                } else {
                    const int rowsel = qd >= 260, q2 = rowsel ? qd - 260 : qd - 100;
                    *reinterpret_cast<u32x2*>(shm + L_XR + ((q2 >> 1) * 2 + rowsel) * 16 + (q2 & 1) * 8) = u32x2{pk2(o[0], o[1]), pk2(o[2], o[3])};
                }
            }
            if (tid == 0) *reinterpret_cast<u32x4*>(shm + L_XR + 2560) = u32x4{0u, 0u, 0u, 0u};
        }
#pragma unroll
        for (int a = 0; a < 5; ++a)
            rx[a] = *reinterpret_cast<const u32x2*>(x + (size_t)trow * D_ + n0 + 16 * a + 4 * g);

        {
            const int moff = L_KB_A + (64 * mh + fr) * 128 + ((u ^ (fr >> 1)) * 16);
            const int noff = L_KB_B + (u * 80 + fr) * 16;
            f32x4 acc[6][4];
#pragma unroll
            for (int a = 0; a < 6; ++a)
#pragma unroll
                for (int b = 0; b < 4; ++b) acc[a][b] = f32x4{0.f, 0.f, 0.f, 0.f};
            bf16x8 mf[2][4], nf[2][5];
            const short one = 0x3F80;
            const bf16x8 ones = {one, one, one, one, one, one, one, one};
            auto read = [&](int st, auto sc_) {
                constexpr int s = decltype(sc_)::value;
                const char* al = shm + moff + (st & 3) * 16384;
                const char* bl = shm + noff + (st & 3) * 10240;
#pragma unroll
                for (int b = 0; b < 4; ++b) mf[s][b] = *reinterpret_cast<const bf16x8*>(al + b * 2048);
#pragma unroll
                for (int a = 0; a < 5; ++a) nf[s][a] = *reinterpret_cast<const bf16x8*>(bl + a * 256);
            };
            auto mma = [&](auto sc_) {
                constexpr int s = decltype(sc_)::value;
#pragma unroll
                for (int b = 0; b < 4; ++b) {
#pragma unroll
                    for (int a = 0; a < 5; ++a) acc[a][b] = MFMA(nf[s][a], mf[s][b], acc[a][b]);
                    acc[5][b] = MFMA(ones, mf[s][b], acc[5][b]);
                }
#pragma unroll
                for (int i = 0; i < 9; ++i) { SGB(0x8, 2); SGB(0x100, 1); }
                SGB(0x8, 6);
            };
            compute_loop<32>(read, mma);
            f32x4* xin = reinterpret_cast<f32x4*>(shm + L_X) + wid * 768 + lane;
            f32x4* xout = reinterpret_cast<f32x4*>(shm + L_X) + (wid ^ 2) * 768 + lane;
            const unsigned sgn = mh ? 0x80008000u : 0u;
            auto epi = [&](auto khc) {
                constexpr int KH = decltype(khc)::value;
#pragma unroll
                for (int b2 = 0; b2 < 2; ++b2)
#pragma unroll
                    for (int a = 0; a < 6; ++a) xout[(b2 * 6 + a) * 64] = acc[a][2 * (1 - KH) + b2];
                BAR_LGKM();
#pragma unroll
                for (int b2 = 0; b2 < 2; ++b2) {
                    const float rsum = acc[5][2 * KH + b2][0] + xin[(b2 * 6 + 5) * 64][0];
                    int t = t0 + 16 * (2 * KH + b2) + fr;
#pragma unroll
                    for (int a = 0; a < 5; ++a) {
                        f32x4 o = xin[(b2 * 6 + a) * 64];
                        f32x4 m = acc[a][2 * KH + b2];
                        f32x4 cc = *reinterpret_cast<const f32x4*>(tab + T_CA + 16 * a + 4 * g);
                        f32x4 ss = *reinterpret_cast<const f32x4*>(tab + T_SA + 16 * a + 4 * g);
                        f32x4 vv = cc * (m + o) + ss * rsum;
                        int kp = 640 * mh + n0 + 16 * a + 4 * g;
                        u32x2 st2 = {pk2(vv[0], vv[1]) ^ sgn, pk2(vv[2], vv[3]) ^ sgn};
                        __builtin_amdgcn_raw_buffer_store_b64(st2, wrs, WS_PQ + ((kp >> 3) * S_ + t) * 16 + (kp & 7) * 2, 0, 16);
                    }
                }
            };
            if (kh) epi(IC<1>{}); else epi(IC<0>{});
            WAIT_V(0);
            if (lane == 0) flag_set(ws, WS_FG + (0 * 32 + rb) * 256 + (cb * 4 + wid) * 8, tag);
            BAR_ONLY();
        }
        {
            const int moff = L_C_A + (u * 64 + 32 * mh + fr) * 16;
            const int noff = L_C_B + fr * 128 + ((u ^ (fr >> 1)) * 16);
            f32x4 acc[5][2];
#pragma unroll
            for (int a = 0; a < 5; ++a)
#pragma unroll
                for (int b = 0; b < 2; ++b) acc[a][b] = f32x4{0.f, 0.f, 0.f, 0.f};
            bf16x8 mf[2][2][2], nf[2][2][5];
            auto read = [&](int st, auto sc_) {
                constexpr int s = decltype(sc_)::value;
                const char* al = shm + moff + (st % 3) * 16384;
                const char* bl = shm + noff + (st % 3) * 20480;
#pragma unroll
                for (int k = 0; k < 2; ++k) {
#pragma unroll
                    for (int b = 0; b < 2; ++b) mf[s][k][b] = *reinterpret_cast<const bf16x8*>(al + k * 8192 + b * 256);
#pragma unroll
                    for (int a = 0; a < 5; ++a) nf[s][k][a] = *reinterpret_cast<const bf16x8*>(bl + k * 10240 + a * 2048);
                }
            };
            auto mma = [&](auto sc_) {
                constexpr int s = decltype(sc_)::value;
#pragma unroll
                for (int k = 0; k < 2; ++k)
#pragma unroll
                    for (int b = 0; b < 2; ++b)
#pragma unroll
                        for (int a = 0; a < 5; ++a) acc[a][b] = MFMA(nf[s][k][a], mf[s][k][b], acc[a][b]);
#pragma unroll
                for (int i = 0; i < 14; ++i) { SGB(0x8, 1); SGB(0x100, 1); }
                SGB(0x8, 6);
            };
            compute_loop<10>(read, mma);
            f32x4* xin = reinterpret_cast<f32x4*>(shm + L_X) + wid * 320 + lane;
            f32x4* xout = reinterpret_cast<f32x4*>(shm + L_X) + (wid ^ 2) * 320 + lane;
            f32x2 xr2[10];
            auto epi = [&](auto khc) {
                constexpr int KH = decltype(khc)::value;
#pragma unroll
                for (int a = 0; a < 5; ++a) xout[a * 64] = acc[a][1 - KH];
                BAR_LGKM();
#pragma unroll
                for (int a = 0; a < 5; ++a) {
                    f32x4 o = xin[a * 64];
                    f32x4 m = acc[a][KH];
                    f32x4 gg = *reinterpret_cast<const f32x4*>(tab + T_GA + 16 * a + 4 * g);
                    f32x4 y = m + o;
                    f32x2 v0 = up2(rx[a][0]) + bfr2(f32x2{gg[0], gg[1]} * bfr2(f32x2{y[0], y[1]}));
                    f32x2 v1 = up2(rx[a][1]) + bfr2(f32x2{gg[2], gg[3]} * bfr2(f32x2{y[2], y[3]}));
                    float vv[4] = {v0[0], v0[1], v1[0], v1[1]};
                    rx[a] = u32x2{pk2(vv[0], vv[1]), pk2(vv[2], vv[3])};
                    f32x4 c4 = *reinterpret_cast<const f32x4*>(tab + T_CB + 16 * a + 4 * g);
                    xr2[2 * a] = up2(rx[a][0]);
                    xr2[2 * a + 1] = up2(rx[a][1]);
                    u32x2 z2 = {pk2v(xr2[2 * a] * f32x2{c4[0], c4[1]}), pk2v(xr2[2 * a + 1] * f32x2{c4[2], c4[3]})};
                    const int e = n0 + 16 * a + 4 * g;
                    __builtin_amdgcn_raw_buffer_store_b64(z2, wrs, WS_X1 + ((e >> 3) * S_ + trow) * 16 + (e & 7) * 2, 0, 16);
                }
            };
            if (kh) epi(IC<1>{}); else epi(IC<0>{});
            {
                f32x2 s1 = xr2[0];
#pragma unroll
                for (int i = 1; i < 10; ++i) s1 += xr2[i];
                const float ml = red_g(s1[0] + s1[1]) * (1.f / 80.f);
                f32x2 s2 = {0.f, 0.f};
#pragma unroll
                for (int i = 0; i < 10; ++i) { f32x2 e = xr2[i] - ml; s2 += e * e; }
                const float m2 = red_g(s2[0] + s2[1]);
                if (g == 0) __builtin_amdgcn_raw_buffer_store_b64(u32x2{__float_as_uint(ml), __float_as_uint(m2)}, wrs, WS_ST + (trow * 8 + cb) * 8, 0, 16);
            }
            WAIT_V(0);
            if (lane == 0) flag_set(ws, WS_FG + (1 * 32 + rb) * 256 + (cb * 4 + wid) * 8, tag);
            BAR_ONLY();
        }
    }

    const int kd_off = (5 * rbl) / 2;
    if (wid >= 5) {
        BAR_ONLY();
        BAR_ONLY();
        if (wid == 5) {
            unsigned aoff[8];
#pragma unroll
            for (int q = 0; q < 8; ++q) aoff[q] = (unsigned)(WS_X1 + (q * S_ + t0 + lane) * 16);
            auto issue = [&](int st) {
                char* sl = shm + L_D_A + (st & 3) * 8192;
                int ch = st + kd_off;
                ch = ch >= 10 ? ch - 10 : ch;
                const char* ag = ws + ch * (8 * S_ * 16);
#pragma unroll
                for (int q = 0; q < 8; ++q) glds16<16>(ag + aoff[q], sl + q * 1024);
            };
            poll_flags<1>(ws, WS_FG + (1 * 32 + rb) * 256, 32, lane, tag);
#pragma nounroll
            for (int i = 0; i < 2; ++i) issue(i);
            stream_run<10, 4, 8, 2>(issue);
        } else {
            const int h = wid - 6;
            unsigned doff[12];
#pragma unroll
            for (int q = 0; q < 12; ++q) {
                int i = (h * 12 + q) * 64 + lane;
                doff[q] = (unsigned)(WS_W1U + ((i / 192) * F_ + cb * 192 + (i % 192)) * 16);
            }
            auto issue = [&](int st) {
                char* sl = shm + L_D_B + (st % 3) * 24576 + h * 12288;
                int ch = st + kd_off;
                ch = ch >= 10 ? ch - 10 : ch;
                const char* bg = ws + ch * (8 * F_ * 16);
#pragma unroll
                for (int q = 0; q < 12; ++q) glds16<0>(bg + doff[q], sl + q * 1024);
            };
#pragma nounroll
            for (int i = 0; i < D_PRE; ++i) issue(i);
            stream_run<10, 3, 12, D_PRE>(issue);
        }
        if (wid == 5) {
            unsigned aoff[8];
#pragma unroll
            for (int q = 0; q < 8; ++q) aoff[q] = (unsigned)(WS_HH + (q * S_ + t0 + lane) * 16);
            auto issue = [&](int st) {
                char* sl = shm + L_C_A + (st % 6) * 8192;
                int ch = st + ke_off;
                ch = ch >= 24 ? ch - 24 : ch;
                const char* ag = ws + ch * (8 * S_ * 16);
#pragma unroll
                for (int q = 0; q < 8; ++q) glds16<16>(ag + aoff[q], sl + q * 1024);
            };
            BAR_ONLY();
            BAR_ONLY();
            poll_flags<1>(ws, WS_FG + (2 * 32 + rb) * 256, 32, lane, tag);
            auto issue2 = [&](int S) { issue(2 * S); issue(2 * S + 1); };
            issue2(0);
            stream_run<12, 3, 16, 1>(issue2);
        } else {
            const int h = wid - 6;
            unsigned boff[5];
#pragma unroll
            for (int q = 0; q < 5; ++q) {
                int i = (h * 5 + q) * 64 + lane;
                boff[q] = (unsigned)(WS_W2U + ((i / 80) * D_ + n0 + (i % 80)) * 16);
            }
            auto issue = [&](int st) {
                char* sl = shm + L_C_B + (st % 6) * 10240 + h * 5120;
                int ch = st + ke_off;
                ch = ch >= 24 ? ch - 24 : ch;
                const char* bg = ws + ch * (8 * D_ * 16);
#pragma unroll
                for (int q = 0; q < 5; ++q) glds16<0>(bg + boff[q], sl + q * 1024);
            };
            BAR_ONLY();
            BAR_ONLY();
            auto issue2 = [&](int S) { issue(2 * S); issue(2 * S + 1); };
#pragma nounroll
            for (int i = 0; i < B_PRE2; ++i) issue2(i);
            stream_run<12, 3, 10, B_PRE2>(issue2);
        }
    } else if (wid == 4) {
        BAR_ONLY();
        BAR_ONLY();
        BAR_ONLY();
        {
            u32x4 st4[4];
#pragma unroll
            for (int i = 0; i < 4; ++i) st4[i] = __builtin_amdgcn_raw_buffer_load_b128(wrs, WS_ST + (t0 + lane) * 64 + i * 16, 0, 16);
            BAR_ONLY(); BAR_ONLY(); BAR_ONLY();
            float mean = 0.f, m2 = 0.f;
#pragma unroll
            for (int i = 0; i < 4; ++i) {
                mean += __uint_as_float(st4[i][0]) + __uint_as_float(st4[i][2]);
                m2 += __uint_as_float(st4[i][1]) + __uint_as_float(st4[i][3]);
            }
            mean *= 0.125f;
#pragma unroll
            for (int i = 0; i < 4; ++i) {
                const float d0 = __uint_as_float(st4[i][0]) - mean, d1 = __uint_as_float(st4[i][2]) - mean;
                m2 += 80.f * (d0 * d0 + d1 * d1);
            }
            const float rstd = 1.0f / sqrtf(m2 * (1.f / D_) + 1e-5f);
            *reinterpret_cast<f32x2*>(shm + L_RS + lane * 8) = f32x2{rstd, -rstd * mean};
            asm volatile("s_waitcnt lgkmcnt(0)" ::: "memory");
        }
#pragma nounroll
        for (int j = 0; j < 8; ++j) BAR_ONLY();
    } else {
        {
            const int nh = mh;
            const int moff = L_D_A + (u * 64 + fr) * 16;
            const int noff = L_D_B + (u * 192 + 96 * nh + fr) * 16;
            const int xoff = fr < 2 ? L_XR + (u * 2 + fr) * 16 : L_XR + 2560;
            const int xstep = fr < 2 ? 256 : 0;
            f32x4 acc[6][5];
#pragma unroll
            for (int a = 0; a < 6; ++a)
#pragma unroll
                for (int b = 0; b < 5; ++b) acc[a][b] = f32x4{0.f, 0.f, 0.f, 0.f};
            bf16x8 mf[2][5], nf[2][6];
            auto read = [&](int st, auto sc_) {
                constexpr int s = decltype(sc_)::value;
                int ch = st + kd_off;
                ch = ch >= 10 ? ch - 10 : ch;
                const char* al = shm + moff + (st & 3) * 8192;
                const char* bl = shm + noff + (st % 3) * 24576;
#pragma unroll
                for (int b = 0; b < 4; ++b) mf[s][b] = *reinterpret_cast<const bf16x8*>(al + b * 256);
                mf[s][4] = *reinterpret_cast<const bf16x8*>(shm + xoff + ch * xstep);
#pragma unroll
                for (int a = 0; a < 6; ++a) nf[s][a] = *reinterpret_cast<const bf16x8*>(bl + a * 256);
            };
            auto mma = [&](auto sc_) {
                constexpr int s = decltype(sc_)::value;
#pragma unroll
                for (int b = 0; b < 5; ++b)
#pragma unroll
                    for (int a = 0; a < 6; ++a) acc[a][b] = MFMA(nf[s][a], mf[s][b], acc[a][b]);
#pragma unroll
                for (int i = 0; i < 11; ++i) { SGB(0x8, 2); SGB(0x100, 1); }
                SGB(0x8, 8);
            };
            compute_loop<10>(read, mma);
            f32x4* xin = reinterpret_cast<f32x4*>(shm + L_X) + wid * 768 + lane;
            f32x4* xout = reinterpret_cast<f32x4*>(shm + L_X) + (wid ^ 2) * 768 + lane;
            float* uv = reinterpret_cast<float*>(shm + L_D_A);
            auto epi = [&](auto khc) {
                constexpr int KH = decltype(khc)::value;
#pragma unroll
                for (int b2 = 0; b2 < 2; ++b2)
#pragma unroll
                    for (int a = 0; a < 6; ++a) xout[(b2 * 6 + a) * 64] = acc[a][2 * (1 - KH) + b2];
                if (fr < 2) {
#pragma unroll
                    for (int a = 0; a < 6; ++a) *reinterpret_cast<f32x4*>(uv + (KH * 2 + fr) * 192 + 96 * nh + 16 * a + 4 * g) = acc[a][4];
                }
                BAR_LGKM();
                f32x2 rs2[2];
#pragma unroll
                for (int b2 = 0; b2 < 2; ++b2) rs2[b2] = *reinterpret_cast<const f32x2*>(shm + L_RS + (16 * (2 * KH + b2) + fr) * 8);
#pragma unroll
                for (int a = 0; a < 6; ++a) {
                    const float* up = uv + 96 * nh + 16 * a + 4 * g;
                    const f32x4 u4 = *reinterpret_cast<const f32x4*>(up) + *reinterpret_cast<const f32x4*>(up + 384);
                    const f32x4 v4 = *reinterpret_cast<const f32x4*>(up + 192) + *reinterpret_cast<const f32x4*>(up + 576);
                    const int f = cb * 192 + 96 * nh + 16 * a + 4 * g;
#pragma unroll
                    for (int b2 = 0; b2 < 2; ++b2) {
                        const int t = t0 + 16 * (2 * KH + b2) + fr;
                        f32x4 gsum = acc[a][2 * KH + b2] + xin[(b2 * 6 + a) * 64];
                        f32x4 vv = gsum * rs2[b2][0] + (u4 * rs2[b2][1] + v4);
                        u32x2 st2 = {pk2(fmaxf(vv[0], 0.f), fmaxf(vv[1], 0.f)), pk2(fmaxf(vv[2], 0.f), fmaxf(vv[3], 0.f))};
                        __builtin_amdgcn_raw_buffer_store_b64(st2, wrs, WS_HH + ((f >> 3) * S_ + t) * 16 + (f & 7) * 2, 0, 16);
                    }
                }
            };
            if (kh) epi(IC<1>{}); else epi(IC<0>{});
            WAIT_V(0);
            if (lane == 0) flag_set(ws, WS_FG + (2 * 32 + rb) * 256 + (cb * 4 + wid) * 8, tag);
            BAR_ONLY();
        }
        {
            const int moff = L_C_A + (u * 64 + 32 * mh + fr) * 16;
            const int noff = L_C_B + (u * 80 + fr) * 16;
            f32x4 acc[5][2];
#pragma unroll
            for (int a = 0; a < 5; ++a)
#pragma unroll
                for (int b = 0; b < 2; ++b) acc[a][b] = f32x4{0.f, 0.f, 0.f, 0.f};
            bf16x8 mf[2][2][2], nf[2][2][5];
            auto read = [&](int st, auto sc_) {
                constexpr int s = decltype(sc_)::value;
                const char* al = shm + moff + (st % 3) * 16384;
                const char* bl = shm + noff + (st % 3) * 20480;
#pragma unroll
                for (int k = 0; k < 2; ++k) {
#pragma unroll
                    for (int b = 0; b < 2; ++b) mf[s][k][b] = *reinterpret_cast<const bf16x8*>(al + k * 8192 + b * 256);
#pragma unroll
                    for (int a = 0; a < 5; ++a) nf[s][k][a] = *reinterpret_cast<const bf16x8*>(bl + k * 10240 + a * 256);
                }
            };
            auto mma = [&](auto sc_) {
                constexpr int s = decltype(sc_)::value;
#pragma unroll
                for (int k = 0; k < 2; ++k)
#pragma unroll
                    for (int b = 0; b < 2; ++b)
#pragma unroll
                        for (int a = 0; a < 5; ++a) acc[a][b] = MFMA(nf[s][k][a], mf[s][k][b], acc[a][b]);
#pragma unroll
                for (int i = 0; i < 14; ++i) { SGB(0x8, 1); SGB(0x100, 1); }
                SGB(0x8, 6);
            };
            compute_loop<12>(read, mma);
            f32x4* xin = reinterpret_cast<f32x4*>(shm + L_X) + wid * 320 + lane;
            f32x4* xout = reinterpret_cast<f32x4*>(shm + L_X) + (wid ^ 2) * 320 + lane;
            auto epi = [&](auto khc) {
                constexpr int KH = decltype(khc)::value;
#pragma unroll
                for (int a = 0; a < 5; ++a) xout[a * 64] = acc[a][1 - KH];
                BAR_LGKM();
#pragma unroll
                for (int a = 0; a < 5; ++a) {
                    f32x4 o = xin[a * 64];
                    f32x4 m = acc[a][KH];
                    f32x4 gg = *reinterpret_cast<const f32x4*>(tab + T_GB + 16 * a + 4 * g);
                    f32x4 y = m + o;
                    f32x2 v0 = up2(rx[a][0]) + bfr2(f32x2{gg[0], gg[1]} * bfr2(f32x2{y[0], y[1]}));
                    f32x2 v1 = up2(rx[a][1]) + bfr2(f32x2{gg[2], gg[3]} * bfr2(f32x2{y[2], y[3]}));
                    float vv[4] = {v0[0], v0[1], v1[0], v1[1]};
                    u32x2 st2 = {pk2(vv[0], vv[1]), pk2(vv[2], vv[3])};
                    *reinterpret_cast<u32x2*>(outp + (size_t)trow * D_ + n0 + 16 * a + 4 * g) = st2;
                }
            };
            if (kh) epi(IC<1>{}); else epi(IC<0>{});
        }
    }
}

extern "C" void kernel_launch(void* const* d_in, const int* in_sizes, int n_in, void* d_out, int out_size,
                              void* d_ws, size_t ws_size, hipStream_t stream) {
    k0_prep<<<256, 576, 0, stream>>>((const u16*)d_in[0], (const u16*)d_in[1], (const u16*)d_in[2], (const u16*)d_in[6],
                                     (const u16*)d_in[7], (const u16*)d_in[8], (const u16*)d_in[9], (char*)d_ws);
    k1_chain<<<256, 512, 0, stream>>>((const u16*)d_in[0], (const u16*)d_in[1], (const u16*)d_in[2],
                                      (const u16*)d_in[3], (const u16*)d_in[4], (u16*)d_out, (char*)d_ws);
}
```

```cpp
#include <hip/hip_runtime.h>
#include <hip/hip_bf16.h>
#include <stdint.h>

typedef __attribute__((ext_vector_type(8))) short bf16x8;
typedef __attribute__((ext_vector_type(4))) float f32x4;
typedef __attribute__((ext_vector_type(4))) unsigned u32x4;
typedef __attribute__((ext_vector_type(2))) unsigned u32x2;
typedef __attribute__((ext_vector_type(2))) __bf16 bf16x2_t;
typedef unsigned short u16;
typedef unsigned long long u64;

constexpr int S_ = 2048, D_ = 640, F_ = 1536;

constexpr unsigned WS_NT   = 0;
constexpr unsigned WS_PQ   = WS_NT + S_ * D_ * 2;
constexpr unsigned WS_X1   = WS_PQ + S_ * 2 * D_ * 2;
constexpr unsigned WS_HH   = WS_X1 + S_ * D_ * 2;
constexpr unsigned WS_W1U  = WS_HH + S_ * F_ * 2;
constexpr unsigned WS_W2U  = WS_W1U + D_ * F_ * 2;
constexpr unsigned WS_MOD  = WS_W2U + D_ * F_ * 2;
constexpr unsigned WS_ST   = WS_MOD + 65536;
constexpr unsigned WS_RS   = WS_ST + S_ * 8 * 8;
constexpr unsigned WS_MX   = WS_RS + 2 * S_ * 4;
constexpr unsigned WS_FG   = WS_MX + S_ * 4;
constexpr unsigned WS_END  = WS_FG + 3 * 32 * 32 * 8;

constexpr int L_KB_A = 0, L_KB_B = 65536;
constexpr int L_C_B = 0, L_C_A = 61440;
constexpr int L_D_B = 0, L_D_A = 73728;
constexpr int L_X = 106496;
constexpr int L_TAB = 157184;
constexpr int T_CA = 0, T_SA = 80, T_GA = 160, T_GB = 240, T_CB = 320;
constexpr int L_XR = L_TAB + 1600;
constexpr int L_RS = L_XR + 2576;
typedef __attribute__((ext_vector_type(4))) short s16x4;
typedef __attribute__((address_space(3))) s16x4* lds_s16x4_ptr;

__device__ __forceinline__ float bf2f(u16 v) { return __uint_as_float(((unsigned)v) << 16); }
__device__ __forceinline__ float bflo(unsigned v) { return __uint_as_float(v << 16); }
__device__ __forceinline__ float bfhi(unsigned v) { return __uint_as_float(v & 0xffff0000u); }
typedef __attribute__((ext_vector_type(2))) float f32x2;
__device__ __forceinline__ unsigned pk2v(f32x2 v) { return __builtin_bit_cast(unsigned, __builtin_convertvector(v, bf16x2_t)); }
__device__ __forceinline__ unsigned pk2(float a, float b) { return pk2v(f32x2{a, b}); }
__device__ __forceinline__ float bfr(float a) { return bflo(pk2(a, a)); }
__device__ __forceinline__ f32x2 up2(unsigned v) { return f32x2{bflo(v), bfhi(v)}; }
__device__ __forceinline__ f32x2 bfr2(f32x2 v) { return up2(pk2v(v)); }
__device__ __forceinline__ float red_g(float v) {
    auto r = __builtin_amdgcn_permlane16_swap(__float_as_uint(v), __float_as_uint(v), false, false);
    const unsigned r0 = r[0], r1 = r[1];
    v = __uint_as_float(r0) + __uint_as_float(r1);
    auto q = __builtin_amdgcn_permlane32_swap(__float_as_uint(v), __float_as_uint(v), false, false);
    const unsigned q0 = q[0], q1 = q[1];
    return __uint_as_float(q0) + __uint_as_float(q1);
}
template <int CTRL>
__device__ __forceinline__ float dpp_add(float v) {
    return v + __builtin_bit_cast(float, __builtin_amdgcn_update_dpp(0, __builtin_bit_cast(int, v), CTRL, 0xf, 0xf, true));
}
__device__ __forceinline__ float red8(float v) {
    v = dpp_add<0xB1>(v); v = dpp_add<0x4E>(v); v = dpp_add<0x141>(v);
    return v;
}
__device__ __forceinline__ float red32(float v) {
    v = red8(v); v = dpp_add<0x140>(v);
    auto r = __builtin_amdgcn_permlane16_swap(__builtin_bit_cast(unsigned, v), __builtin_bit_cast(unsigned, v), false, false);
    const unsigned r0 = r[0], r1 = r[1];
    return __uint_as_float(r0) + __uint_as_float(r1);
}

constexpr int B_PRE2 = 2, D_PRE = 2;
template <int V> struct IC { static constexpr int value = V; };
#define WAIT_V(n) asm volatile("s_waitcnt vmcnt(%0)" ::"n"(n) : "memory")
#define BAR_ONLY() asm volatile("s_barrier" ::: "memory")
#define BAR_LGKM() do { __builtin_amdgcn_sched_barrier(0); asm volatile("" ::: "memory"); __builtin_amdgcn_s_waitcnt(0xC07F); \
    __builtin_amdgcn_s_barrier(); asm volatile("" ::: "memory"); __builtin_amdgcn_sched_barrier(0); } while (0)
#define MFMA(a, b, c) __builtin_amdgcn_mfma_f32_16x16x32_bf16(a, b, c, 0, 0, 0)
#define SGB(mask, n) __builtin_amdgcn_sched_group_barrier(mask, n, 0)

template <int AUX>
__device__ __forceinline__ void glds16(const char* g, char* l) {
    __builtin_amdgcn_global_load_lds((const unsigned*)g, (unsigned*)l, 16, 0, AUX);
}
__device__ __forceinline__ void flag_set(char* ws, unsigned off, u64 tag) {
    __hip_atomic_store(reinterpret_cast<u64*>(ws + off), tag, __ATOMIC_RELAXED, __HIP_MEMORY_SCOPE_AGENT);
}
__device__ __forceinline__ u64 flag_get(const char* ws, unsigned off) {
    return __hip_atomic_load(reinterpret_cast<const u64*>(ws + off), __ATOMIC_RELAXED, __HIP_MEMORY_SCOPE_AGENT);
}
template <int NPL>
__device__ __forceinline__ void poll_flags(const char* ws, unsigned off, int nflags, int lane, u64 tag) {
    for (int spin = 0; spin < (1 << 20); ++spin) {
        bool ok = true;
#pragma unroll
        for (int q = 0; q < NPL; ++q) {
            int i = q * 64 + lane;
            u64 v = flag_get(ws, off + (i < nflags ? i : 0) * 8);
            ok &= (v == tag);
        }
        if (__all(ok)) break;
        __builtin_amdgcn_s_sleep(2);
    }
    asm volatile("" ::: "memory");
}

template <int NT, int G, int J>
__device__ __forceinline__ void stream_tail() {
    if constexpr (J < NT) {
        constexpr int rem = (NT - 2 - J) > 0 ? (NT - 2 - J) : 0;
        WAIT_V(rem * G);
        BAR_ONLY();
        stream_tail<NT, G, J + 1>();
    }
}
template <int NT, int NSL, int G, int PRE, class Issue>
__device__ __forceinline__ void stream_run(Issue& issue) {
    WAIT_V((PRE - 1) * G);
    BAR_ONLY();
#pragma nounroll
    for (int i = PRE; i < NSL; ++i) issue(i);
#pragma nounroll
    for (int j = 0; j < NT - NSL; ++j) {
        WAIT_V((NSL - 2) * G);
        BAR_ONLY();
        issue(j + NSL);
    }
    stream_tail<NT, G, NT - NSL>();
}

template <int NT, class Read, class Mma>
__device__ __forceinline__ void compute_loop(Read& read, Mma& mma) {
    BAR_LGKM();
    read(0, IC<0>{});
#pragma nounroll
    for (int j = 0; j < NT; j += 2) {
        BAR_LGKM();
        read(j + 1, IC<1>{});
        mma(IC<0>{});
        BAR_LGKM();
        read(j + 2 < NT ? j + 2 : j, IC<0>{});
        mma(IC<1>{});
    }
}

__global__ __launch_bounds__(768) void k0_prep(const u16* __restrict__ x, const u16* __restrict__ wsre,
                                               const u16* __restrict__ wsim, const u16* __restrict__ w1,
                                               const u16* __restrict__ w2, const u16* __restrict__ cvec,
                                               const u16* __restrict__ wmod, char* __restrict__ ws) {
    __shared__ __attribute__((aligned(1024))) char shm[16384 + 10752 + 2048];
    const int tid = threadIdx.x, lane = tid & 63;
    const int wid = __builtin_amdgcn_readfirstlane(tid >> 6);
    const int bid = blockIdx.x;
    if (wid >= 8) {
        const int rb = (bid & 7) * 4 + (bid >> 6), cb = (bid >> 3) & 7;
        const int mat = cb >> 2, trow = rb * 64 + ((16 * cb) & 63) + 4 * (wid - 8);
        const u32x4* src = reinterpret_cast<const u32x4*>((mat ? wsim : wsre) + (size_t)trow * S_) + lane;
        u32x4 v[16];
#pragma unroll
        for (int i = 0; i < 16; ++i) v[i] = src[((i & 3) * 4 + (i >> 2)) * 64];
        BAR_ONLY();
        float r4[4];
#pragma unroll
        for (int r = 0; r < 4; ++r) {
            f32x2 a = {0.f, 0.f};
#pragma unroll
            for (int i = 0; i < 4; ++i)
#pragma unroll
                for (int j = 0; j < 4; ++j) a += up2(v[4 * i + r][j]);
            r4[r] = red_g(dpp_add<0x140>(red8(a[0] + a[1])));
        }
        if (lane == 0) *reinterpret_cast<f32x4*>(ws + WS_RS + (mat * S_ + trow) * 4) = f32x4{r4[0], r4[1], r4[2], r4[3]};
        return;
    }
    if (wid >= 6) {
        if (bid >= 240) return;
        const int wt = bid * 2 + (wid - 6);
        const bool isw1 = wt < 240;
        const int wl = isw1 ? wt : wt - 240;
        const int N = isw1 ? F_ : D_, t = wl * 64 + lane, nbn = isw1 ? 192 : 80;
        const int kb = t / nbn, nb = t % nbn;
        const u16* src = isw1 ? w1 : w2;
        u32x4 w[8];
#pragma unroll
        for (int k = 0; k < 8; ++k)
            w[k] = __builtin_nontemporal_load(reinterpret_cast<const u32x4*>(src + (size_t)(8 * kb + k) * N + 8 * nb));
        BAR_ONLY();
        char* sr = shm + (wid - 6) * 8192;
#pragma unroll
        for (int n = 0; n < 8; ++n) {
            u32x4 o;
#pragma unroll
            for (int p = 0; p < 4; ++p) {
                unsigned a = w[2 * p][n >> 1], b = w[2 * p + 1][n >> 1];
                o[p] = (n & 1) ? ((a >> 16) | (b & 0xffff0000u)) : ((a & 0xffffu) | (b << 16));
            }
            *reinterpret_cast<u32x4*>(sr + lane * 128 + ((n ^ (lane & 7)) * 16)) = o;
        }
        char* ob = ws + (isw1 ? WS_W1U : WS_W2U) + (size_t)(wl * 512) * 16;
#pragma unroll
        for (int i = 0; i < 8; ++i) {
            u32x4 o = *reinterpret_cast<const u32x4*>(sr + (8 * i + (lane >> 3)) * 128 + (((lane & 7) ^ ((lane >> 3) & 7)) * 16));
            *reinterpret_cast<u32x4*>(ob + (i * 64 + lane) * 16) = o;
        }
        return;
    }
    if (wid >= 4) {
        if (bid >= 240) return;
        const int hw = wid - 4, cg = bid % 60, ks = bid / 60, rg = lane >> 3, cl = lane & 7;
        const int r0 = ks * 160 + hw * 80;
        u16 cv[2];
#pragma unroll
        for (int p = 0; p < 2; ++p) { int r = p * 64 + lane; cv[p] = cvec[r0 + (r < 80 ? r : 0)]; }
        u32x4 w[10];
#pragma unroll
        for (int p = 0; p < 10; ++p)
            w[p] = __builtin_nontemporal_load(reinterpret_cast<const u32x4*>(wmod + (size_t)(r0 + p * 8 + rg) * 3840 + cg * 64 + cl * 8));
        BAR_ONLY();
        float* sl = reinterpret_cast<float*>(shm + 16384 + 10752) + hw * 128;
#pragma unroll
        for (int p = 0; p < 2; ++p) {
            float c1 = bf2f(cv[p]);
            sl[p * 64 + lane] = bfr(c1 / (1.f + __expf(-c1)));
        }
        f32x2 a[4];
#pragma unroll
        for (int j = 0; j < 4; ++j) a[j] = f32x2{0.f, 0.f};
#pragma unroll
        for (int p = 0; p < 10; ++p) {
            const float sv = sl[p * 8 + rg];
#pragma unroll
            for (int j = 0; j < 4; ++j) a[j] += up2(w[p][j]) * sv;
        }
        float r8[8] = {a[0][0], a[0][1], a[1][0], a[1][1], a[2][0], a[2][1], a[3][0], a[3][1]};
#pragma unroll
        for (int j = 0; j < 8; ++j) r8[j] = red_g(dpp_add<0x128>(r8[j]));
        f32x4* cmb = reinterpret_cast<f32x4*>(shm + 16384 + 10752 + 1024);
        const f32x4 o = lane < 8 ? f32x4{r8[0], r8[1], r8[2], r8[3]} : f32x4{r8[4], r8[5], r8[6], r8[7]};
        if (hw == 1 && lane < 16) cmb[lane] = o;
        BAR_LGKM();
        if (hw == 0 && lane < 16)
            *reinterpret_cast<f32x4*>(ws + WS_MOD + (ks * 3840 + cg * 64 + cl * 8 + (lane >> 3) * 4) * 4) = o + cmb[lane];
        return;
    }
    if (tid < 12) reinterpret_cast<u64*>(ws + WS_FG)[bid * 12 + tid] = 0ull;
    const int row = 2 * wid + (lane >> 5), l32 = lane & 31;
    const u16* xr = x + (size_t)(bid * 8 + row) * D_;
    u32x2 v[5];
#pragma unroll
    for (int i = 0; i < 5; ++i) v[i] = __builtin_nontemporal_load(reinterpret_cast<const u32x2*>(xr + (l32 + 32 * i) * 4));
    const float c_lo = bflo(__builtin_amdgcn_readlane(v[0][0], 0)), c_hi = bflo(__builtin_amdgcn_readlane(v[0][0], 32));
    const float c0 = lane < 32 ? c_lo : c_hi;
    f32x2 d[10];
    f32x2 s1 = {0.f, 0.f}, s2 = {0.f, 0.f};
#pragma unroll
    for (int i = 0; i < 5; ++i)
#pragma unroll
        for (int j = 0; j < 2; ++j) {
            f32x2 e = up2(v[i][j]) - c0;
            d[2 * i + j] = e;
            s1 += e;
            s2 += e * e;
        }
    const float m1 = red32(s1[0] + s1[1]) * (1.f / D_);
    const float m2 = red32(s2[0] + s2[1]) * (1.f / D_);
    const float rstd = 1.0f / sqrtf(fmaxf(m2 - m1 * m1, 0.f) + 1e-5f);
    const float nb_ = -m1 * rstd;
    if (l32 == 0) *reinterpret_cast<float*>(ws + WS_MX + (bid * 8 + row) * 4) = m1 + c0;
    char* sc = shm + 16384;
#pragma unroll
    for (int i = 0; i < 5; ++i) {
        u32x2 o = {pk2v(d[2 * i] * rstd + nb_), pk2v(d[2 * i + 1] * rstd + nb_)};
        *reinterpret_cast<u32x2*>(sc + row * 1344 + (l32 + 32 * i) * 8) = o;
    }
    BAR_LGKM();
    const int tq = (lane >> 2) & 3, tp = lane & 3, tg = lane >> 4;
#pragma unroll
    for (int k = 0; k < 3; ++k) {
        const int i = wid + 4 * k;
        if (i < 10) {
            const char* a0 = sc + tq * 1344 + (64 * i + 16 * tg + 4 * tp) * 2;
            s16x4 lo = __builtin_amdgcn_ds_read_tr16_b64_v4i16((lds_s16x4_ptr)(a0));
            s16x4 hi = __builtin_amdgcn_ds_read_tr16_b64_v4i16((lds_s16x4_ptr)(a0 + 4 * 1344));
            u32x2 l2 = __builtin_bit_cast(u32x2, lo), h2 = __builtin_bit_cast(u32x2, hi);
            *reinterpret_cast<u32x4*>(ws + WS_NT + (bid * D_ + 64 * i + lane) * 16) = u32x4{l2[0], l2[1], h2[0], h2[1]};
        }
    }
}

__global__ __launch_bounds__(512, 2) void k1_chain(const u16* __restrict__ x, const u16* __restrict__ wsre,
                                                   const u16* __restrict__ wsim, const u16* __restrict__ wdre,
                                                   const u16* __restrict__ wdim, u16* __restrict__ outp,
                                                   char* __restrict__ ws) {
    __shared__ __attribute__((aligned(1024))) char shm[163840];
    const int tid = threadIdx.x, lane = tid & 63;
    const int wid = __builtin_amdgcn_readfirstlane(tid >> 6);
    const int bid = blockIdx.x;
    const int rb = (bid & 7) * 4 + (bid >> 6), cb = (bid >> 3) & 7, rbl = bid >> 6;
    const int t0 = rb * 64, n0 = cb * 80;
    const auto wrs = __builtin_amdgcn_make_buffer_rsrc(ws, 0, WS_END, 0x00020000);
    const float* tab = reinterpret_cast<const float*>(shm + L_TAB);
    constexpr u64 tag = 1ull;
    const int mh = wid & 1, kh = (wid >> 1) & 1, fr = lane & 15, g = lane >> 4;
    const int u = 4 * kh + g;
    const int trow = t0 + 32 * mh + 16 * kh + fr;
    u32x2 rx[5];
    const int kc_off = 5 * rbl, ke_off = 6 * rbl;
    auto kb_chunk = [&](int st) { return (st & ~7) | ((st + 2 * rbl) & 7); };

    if (wid >= 5) {
        if (wid < 7) {
            const int h = wid - 5;
            const char* abase = reinterpret_cast<const char*>(h ? wsim : wsre);
            unsigned aoff[8];
#pragma unroll
            for (int q = 0; q < 8; ++q) {
                int r = 8 * q + (lane >> 3);
                aoff[q] = (unsigned)((t0 + r) * 4096 + (((lane & 7) ^ ((r >> 1) & 7)) * 16));
            }
            auto issue = [&](int st) {
                char* sl = shm + L_KB_A + (st & 3) * 16384 + h * 8192;
                const char* ag = abase + kb_chunk(st) * 128;
#pragma unroll
                for (int q = 0; q < 8; ++q) glds16<2>(ag + aoff[q], sl + q * 1024);
            };
#pragma nounroll
            for (int i = 0; i < 2; ++i) issue(i);
            stream_run<32, 4, 8, 2>(issue);
        } else {
            unsigned boff[10];
#pragma unroll
            for (int q = 0; q < 10; ++q) {
                int i = q * 64 + lane;
                boff[q] = (unsigned)(WS_NT + ((i / 80) * D_ + n0 + (i % 80)) * 16);
            }
            auto issue = [&](int st) {
                char* sl = shm + L_KB_B + (st & 3) * 10240;
                const char* bg = ws + kb_chunk(st) * (8 * D_ * 16);
#pragma unroll
                for (int q = 0; q < 10; ++q) glds16<0>(bg + boff[q], sl + q * 1024);
            };
#pragma nounroll
            for (int i = 0; i < 2; ++i) issue(i);
            stream_run<32, 4, 10, 2>(issue);
        }
        if (wid == 5) {
            unsigned aoff[8];
#pragma unroll
            for (int q = 0; q < 8; ++q) aoff[q] = (unsigned)(WS_PQ + (q * S_ + t0 + lane) * 16);
            auto issue = [&](int st) {
                char* sl = shm + L_C_A + (st % 6) * 8192;
                int ch = st + kc_off;
                ch = ch >= 20 ? ch - 20 : ch;
                const char* ag = ws + ch * (8 * S_ * 16);
#pragma unroll
                for (int q = 0; q < 8; ++q) glds16<16>(ag + aoff[q], sl + q * 1024);
            };
            BAR_ONLY();
            BAR_ONLY();
            poll_flags<1>(ws, WS_FG + (0 * 32 + rb) * 256, 32, lane, tag);
            auto issue2 = [&](int S) { issue(2 * S); issue(2 * S + 1); };
            issue2(0);
            stream_run<10, 3, 16, 1>(issue2);
        } else {
            const int h = wid - 6;
            unsigned boff[5];
#pragma unroll
            for (int q = 0; q < 5; ++q) {
                int r = 40 * h + 8 * q + (lane >> 3);
                boff[q] = (unsigned)((n0 + r) * 1280 + (((lane & 7) ^ ((r >> 1) & 7)) * 16));
            }
            auto issue = [&](int st) {
                char* sl = shm + L_C_B + (st % 6) * 10240 + h * 5120;
                int ch = st + kc_off;
                ch = ch >= 20 ? ch - 20 : ch;
                const char* bg = ch < 10 ? reinterpret_cast<const char*>(wdre) + ch * 128
                                         : reinterpret_cast<const char*>(wdim) + (ch - 10) * 128;
#pragma unroll
                for (int q = 0; q < 5; ++q) glds16<0>(bg + boff[q], sl + q * 1024);
            };
            BAR_ONLY();
            BAR_ONLY();
            auto issue2 = [&](int S) { issue(2 * S); issue(2 * S + 1); };
#pragma nounroll
            for (int i = 0; i < B_PRE2; ++i) issue2(i);
            stream_run<10, 3, 10, B_PRE2>(issue2);
        }
    } else if (wid == 4) {
#pragma nounroll
        for (int j = 0; j < 33; ++j) BAR_ONLY();
        BAR_ONLY();
        BAR_ONLY();
#pragma nounroll
        for (int j = 0; j < 11; ++j) BAR_ONLY();
    } else {
        {
            const float* part = reinterpret_cast<const float*>(ws + WS_MOD);
            f32x4 p[2][4];
#pragma unroll
            for (int it = 0; it < 2; ++it) {
                int qd = it * 256 + tid;
                qd = qd < 420 ? qd : 419;
                int col = qd < 20 ? 640 + n0 + qd * 4 : qd < 40 ? n0 + (qd - 20) * 4 : qd < 60 ? 1280 + n0 + (qd - 40) * 4
                        : qd < 80 ? 3200 + n0 + (qd - 60) * 4 : qd < 100 ? 2560 + n0 + (qd - 80) * 4
                        : qd < 260 ? 2560 + (qd - 100) * 4 : 1920 + (qd - 260) * 4;
#pragma unroll
                for (int k = 0; k < 4; ++k) p[it][k] = *reinterpret_cast<const f32x4*>(part + k * 3840 + col);
            }
#pragma unroll
            for (int it = 0; it < 2; ++it) {
                int qd = it * 256 + tid;
                qd = qd < 420 ? qd : 419;
                const bool addone = qd < 20 || (qd >= 80 && qd < 260);
                f32x4 o;
#pragma unroll
                for (int r = 0; r < 4; ++r) {
                    float m = bfr((p[it][0][r] + p[it][1][r]) + (p[it][2][r] + p[it][3][r]));
                    o[r] = bfr(addone ? 1.f + m : m);
                }
                if (qd < 100) {
                    *reinterpret_cast<f32x4*>(shm + L_TAB + qd * 16) = o;
                } else {
                    const int rowsel = qd >= 260, q2 = rowsel ? qd - 260 : qd - 100;
                    *reinterpret_cast<u32x2*>(shm + L_XR + ((q2 >> 1) * 2 + rowsel) * 16 + (q2 & 1) * 8) = u32x2{pk2(o[0], o[1]), pk2(o[2], o[3])};
                }
            }
            if (tid == 0) *reinterpret_cast<u32x4*>(shm + L_XR + 2560) = u32x4{0u, 0u, 0u, 0u};
        }
#pragma unroll
        for (int a = 0; a < 5; ++a)
            rx[a] = *reinterpret_cast<const u32x2*>(x + (size_t)trow * D_ + n0 + 16 * a + 4 * g);
        const float mx = *reinterpret_cast<const float*>(ws + WS_MX + trow * 4);
        float rsum2[2];
#pragma unroll
        for (int b2 = 0; b2 < 2; ++b2)
            rsum2[b2] = *reinterpret_cast<const float*>(ws + WS_RS + (mh * S_ + t0 + 16 * (2 * kh + b2) + fr) * 4);

        {
            const int moff = L_KB_A + (64 * mh + fr) * 128 + ((u ^ (fr >> 1)) * 16);
            const int noff = L_KB_B + (u * 80 + fr) * 16;
            f32x4 acc[5][4];
#pragma unroll
            for (int a = 0; a < 5; ++a)
#pragma unroll
                for (int b = 0; b < 4; ++b) acc[a][b] = f32x4{0.f, 0.f, 0.f, 0.f};
            bf16x8 mf[2][4], nf[2][5];
            auto read = [&](int st, auto sc_) {
                constexpr int s = decltype(sc_)::value;
                const char* al = shm + moff + (st & 3) * 16384;
                const char* bl = shm + noff + (st & 3) * 10240;
#pragma unroll
                for (int b = 0; b < 4; ++b) mf[s][b] = *reinterpret_cast<const bf16x8*>(al + b * 2048);
#pragma unroll
                for (int a = 0; a < 5; ++a) nf[s][a] = *reinterpret_cast<const bf16x8*>(bl + a * 256);
            };
            auto mma = [&](auto sc_) {
                constexpr int s = decltype(sc_)::value;
#pragma unroll
                for (int b = 0; b < 4; ++b) {
#pragma unroll
                    for (int a = 0; a < 5; ++a) acc[a][b] = MFMA(nf[s][a], mf[s][b], acc[a][b]);
                }
#pragma unroll
                for (int i = 0; i < 9; ++i) { SGB(0x8, 2); SGB(0x100, 1); }
                SGB(0x8, 2);
            };
            compute_loop<32>(read, mma);
            f32x4* xin = reinterpret_cast<f32x4*>(shm + L_X) + wid * 768 + lane;
            f32x4* xout = reinterpret_cast<f32x4*>(shm + L_X) + (wid ^ 2) * 768 + lane;
            const unsigned sgn = mh ? 0x80008000u : 0u;
            auto epi = [&](auto khc) {
                constexpr int KH = decltype(khc)::value;
#pragma unroll
                for (int b2 = 0; b2 < 2; ++b2)
#pragma unroll
                    for (int a = 0; a < 5; ++a) xout[(b2 * 6 + a) * 64] = acc[a][2 * (1 - KH) + b2];
                BAR_LGKM();
#pragma unroll
                for (int b2 = 0; b2 < 2; ++b2) {
                    const float rsum = rsum2[b2];
                    int t = t0 + 16 * (2 * KH + b2) + fr;
#pragma unroll
                    for (int a = 0; a < 5; ++a) {
                        f32x4 o = xin[(b2 * 6 + a) * 64];
                        f32x4 m = acc[a][2 * KH + b2];
                        f32x4 cc = *reinterpret_cast<const f32x4*>(tab + T_CA + 16 * a + 4 * g);
                        f32x4 ss = *reinterpret_cast<const f32x4*>(tab + T_SA + 16 * a + 4 * g);
                        f32x4 vv = cc * (m + o) + ss * rsum;
                        int kp = 640 * mh + n0 + 16 * a + 4 * g;
                        u32x2 st2 = {pk2(vv[0], vv[1]) ^ sgn, pk2(vv[2], vv[3]) ^ sgn};
                        __builtin_amdgcn_raw_buffer_store_b64(st2, wrs, WS_PQ + ((kp >> 3) * S_ + t) * 16 + (kp & 7) * 2, 0, 16);
                    }
                }
            };
            if (kh) epi(IC<1>{}); else epi(IC<0>{});
            WAIT_V(0);
            if (lane == 0) flag_set(ws, WS_FG + (0 * 32 + rb) * 256 + (cb * 4 + wid) * 8, tag);
            BAR_ONLY();
        }
        {
            const int moff = L_C_A + (u * 64 + 32 * mh + fr) * 16;
            const int noff = L_C_B + fr * 128 + ((u ^ (fr >> 1)) * 16);
            f32x4 acc[5][2];
#pragma unroll
            for (int a = 0; a < 5; ++a)
#pragma unroll
                for (int b = 0; b < 2; ++b) acc[a][b] = f32x4{0.f, 0.f, 0.f, 0.f};
            bf16x8 mf[2][2][2], nf[2][2][5];
            auto read = [&](int st, auto sc_) {
                constexpr int s = decltype(sc_)::value;
                const char* al = shm + moff + (st % 3) * 16384;
                const char* bl = shm + noff + (st % 3) * 20480;
#pragma unroll
                for (int k = 0; k < 2; ++k) {
#pragma unroll
                    for (int b = 0; b < 2; ++b) mf[s][k][b] = *reinterpret_cast<const bf16x8*>(al + k * 8192 + b * 256);
#pragma unroll
                    for (int a = 0; a < 5; ++a) nf[s][k][a] = *reinterpret_cast<const bf16x8*>(bl + k * 10240 + a * 2048);
                }
            };
            auto mma = [&](auto sc_) {
                constexpr int s = decltype(sc_)::value;
#pragma unroll
                for (int k = 0; k < 2; ++k)
#pragma unroll
                    for (int b = 0; b < 2; ++b)
#pragma unroll
                        for (int a = 0; a < 5; ++a) acc[a][b] = MFMA(nf[s][k][a], mf[s][k][b], acc[a][b]);
#pragma unroll
                for (int i = 0; i < 14; ++i) { SGB(0x8, 1); SGB(0x100, 1); }
                SGB(0x8, 6);
            };
            compute_loop<10>(read, mma);
            f32x4* xin = reinterpret_cast<f32x4*>(shm + L_X) + wid * 320 + lane;
            f32x4* xout = reinterpret_cast<f32x4*>(shm + L_X) + (wid ^ 2) * 320 + lane;
            f32x2 xr2[10];
            auto epi = [&](auto khc) {
                constexpr int KH = decltype(khc)::value;
#pragma unroll
                for (int a = 0; a < 5; ++a) xout[a * 64] = acc[a][1 - KH];
                BAR_LGKM();
#pragma unroll
                for (int a = 0; a < 5; ++a) {
                    f32x4 o = xin[a * 64];
                    f32x4 m = acc[a][KH];
                    f32x4 gg = *reinterpret_cast<const f32x4*>(tab + T_GA + 16 * a + 4 * g);
                    f32x4 y = m + o;
                    f32x2 v0 = up2(rx[a][0]) + bfr2(f32x2{gg[0], gg[1]} * bfr2(f32x2{y[0], y[1]}));
                    f32x2 v1 = up2(rx[a][1]) + bfr2(f32x2{gg[2], gg[3]} * bfr2(f32x2{y[2], y[3]}));
                    float vv[4] = {v0[0], v0[1], v1[0], v1[1]};
                    rx[a] = u32x2{pk2(vv[0], vv[1]), pk2(vv[2], vv[3])};
                    f32x4 c4 = *reinterpret_cast<const f32x4*>(tab + T_CB + 16 * a + 4 * g);
                    xr2[2 * a] = up2(rx[a][0]) - mx;
                    xr2[2 * a + 1] = up2(rx[a][1]) - mx;
                    u32x2 z2 = {pk2v(xr2[2 * a] * f32x2{c4[0], c4[1]}), pk2v(xr2[2 * a + 1] * f32x2{c4[2], c4[3]})};
                    const int e = n0 + 16 * a + 4 * g;
                    __builtin_amdgcn_raw_buffer_store_b64(z2, wrs, WS_X1 + ((e >> 3) * S_ + trow) * 16 + (e & 7) * 2, 0, 16);
                }
            };
            if (kh) epi(IC<1>{}); else epi(IC<0>{});
            {
                f32x2 s1 = xr2[0];
#pragma unroll
                for (int i = 1; i < 10; ++i) s1 += xr2[i];
                const float ml = red_g(s1[0] + s1[1]) * (1.f / 80.f);
                f32x2 s2 = {0.f, 0.f};
#pragma unroll
                for (int i = 0; i < 10; ++i) { f32x2 e = xr2[i] - ml; s2 += e * e; }
                const float m2 = red_g(s2[0] + s2[1]);
                if (g == 0) __builtin_amdgcn_raw_buffer_store_b64(u32x2{__float_as_uint(ml), __float_as_uint(m2)}, wrs, WS_ST + (trow * 8 + cb) * 8, 0, 16);
            }
            WAIT_V(0);
            if (lane == 0) flag_set(ws, WS_FG + (1 * 32 + rb) * 256 + (cb * 4 + wid) * 8, tag);
            BAR_ONLY();
        }
    }

    const int kd_off = (5 * rbl) / 2;
    if (wid >= 5) {
        BAR_ONLY();
        BAR_ONLY();
        if (wid == 5) {
            unsigned aoff[8];
#pragma unroll
            for (int q = 0; q < 8; ++q) aoff[q] = (unsigned)(WS_X1 + (q * S_ + t0 + lane) * 16);
            auto issue = [&](int st) {
                char* sl = shm + L_D_A + (st & 3) * 8192;
                int ch = st + kd_off;
                ch = ch >= 10 ? ch - 10 : ch;
                const char* ag = ws + ch * (8 * S_ * 16);
#pragma unroll
                for (int q = 0; q < 8; ++q) glds16<16>(ag + aoff[q], sl + q * 1024);
            };
            poll_flags<1>(ws, WS_FG + (1 * 32 + rb) * 256, 32, lane, tag);
#pragma nounroll
            for (int i = 0; i < 2; ++i) issue(i);
            stream_run<10, 4, 8, 2>(issue);
        } else {
            const int h = wid - 6;
            unsigned doff[12];
#pragma unroll
            for (int q = 0; q < 12; ++q) {
                int i = (h * 12 + q) * 64 + lane;
                doff[q] = (unsigned)(WS_W1U + ((i / 192) * F_ + cb * 192 + (i % 192)) * 16);
            }
            auto issue = [&](int st) {
                char* sl = shm + L_D_B + (st % 3) * 24576 + h * 12288;
                int ch = st + kd_off;
                ch = ch >= 10 ? ch - 10 : ch;
                const char* bg = ws + ch * (8 * F_ * 16);
#pragma unroll
                for (int q = 0; q < 12; ++q) glds16<0>(bg + doff[q], sl + q * 1024);
            };
#pragma nounroll
            for (int i = 0; i < D_PRE; ++i) issue(i);
            stream_run<10, 3, 12, D_PRE>(issue);
        }
        if (wid == 5) {
            unsigned aoff[8];
#pragma unroll
            for (int q = 0; q < 8; ++q) aoff[q] = (unsigned)(WS_HH + (q * S_ + t0 + lane) * 16);
            auto issue = [&](int st) {
                char* sl = shm + L_C_A + (st % 6) * 8192;
                int ch = st + ke_off;
                ch = ch >= 24 ? ch - 24 : ch;
                const char* ag = ws + ch * (8 * S_ * 16);
#pragma unroll
                for (int q = 0; q < 8; ++q) glds16<16>(ag + aoff[q], sl + q * 1024);
            };
            BAR_ONLY();
            BAR_ONLY();
            poll_flags<1>(ws, WS_FG + (2 * 32 + rb) * 256, 32, lane, tag);
            auto issue2 = [&](int S) { issue(2 * S); issue(2 * S + 1); };
            issue2(0);
            stream_run<12, 3, 16, 1>(issue2);
        } else {
            const int h = wid - 6;
            unsigned boff[5];
#pragma unroll
            for (int q = 0; q < 5; ++q) {
                int i = (h * 5 + q) * 64 + lane;
                boff[q] = (unsigned)(WS_W2U + ((i / 80) * D_ + n0 + (i % 80)) * 16);
            }
            auto issue = [&](int st) {
                char* sl = shm + L_C_B + (st % 6) * 10240 + h * 5120;
                int ch = st + ke_off;
                ch = ch >= 24 ? ch - 24 : ch;
                const char* bg = ws + ch * (8 * D_ * 16);
#pragma unroll
                for (int q = 0; q < 5; ++q) glds16<0>(bg + boff[q], sl + q * 1024);
            };
            BAR_ONLY();
            BAR_ONLY();
            auto issue2 = [&](int S) { issue(2 * S); issue(2 * S + 1); };
#pragma nounroll
            for (int i = 0; i < B_PRE2; ++i) issue2(i);
            stream_run<12, 3, 10, B_PRE2>(issue2);
        }
    } else if (wid == 4) {
        BAR_ONLY();
        BAR_ONLY();
        BAR_ONLY();
        {
            u32x4 st4[4];
#pragma unroll
            for (int i = 0; i < 4; ++i) st4[i] = __builtin_amdgcn_raw_buffer_load_b128(wrs, WS_ST + (t0 + lane) * 64 + i * 16, 0, 16);
            BAR_ONLY(); BAR_ONLY(); BAR_ONLY();
            float mean = 0.f, m2 = 0.f;
#pragma unroll
            for (int i = 0; i < 4; ++i) {
                mean += __uint_as_float(st4[i][0]) + __uint_as_float(st4[i][2]);
                m2 += __uint_as_float(st4[i][1]) + __uint_as_float(st4[i][3]);
            }
            mean *= 0.125f;
#pragma unroll
            for (int i = 0; i < 4; ++i) {
                const float d0 = __uint_as_float(st4[i][0]) - mean, d1 = __uint_as_float(st4[i][2]) - mean;
                m2 += 80.f * (d0 * d0 + d1 * d1);
            }
            const float rstd = 1.0f / sqrtf(m2 * (1.f / D_) + 1e-5f);
            *reinterpret_cast<f32x2*>(shm + L_RS + lane * 8) = f32x2{rstd, -rstd * mean};
            asm volatile("s_waitcnt lgkmcnt(0)" ::: "memory");
        }
#pragma nounroll
        for (int j = 0; j < 8; ++j) BAR_ONLY();
    } else {
        {
            const int nh = mh;
            const int moff = L_D_A + (u * 64 + fr) * 16;
            const int noff = L_D_B + (u * 192 + 96 * nh + fr) * 16;
            const int xoff = fr < 2 ? L_XR + (u * 2 + fr) * 16 : L_XR + 2560;
            const int xstep = fr < 2 ? 256 : 0;
            f32x4 acc[6][5];
#pragma unroll
            for (int a = 0; a < 6; ++a)
#pragma unroll
                for (int b = 0; b < 5; ++b) acc[a][b] = f32x4{0.f, 0.f, 0.f, 0.f};
            bf16x8 mf[2][5], nf[2][6];
            auto read = [&](int st, auto sc_) {
                constexpr int s = decltype(sc_)::value;
                int ch = st + kd_off;
                ch = ch >= 10 ? ch - 10 : ch;
                const char* al = shm + moff + (st & 3) * 8192;
                const char* bl = shm + noff + (st % 3) * 24576;
#pragma unroll
                for (int b = 0; b < 4; ++b) mf[s][b] = *reinterpret_cast<const bf16x8*>(al + b * 256);
                mf[s][4] = *reinterpret_cast<const bf16x8*>(shm + xoff + ch * xstep);
#pragma unroll
                for (int a = 0; a < 6; ++a) nf[s][a] = *reinterpret_cast<const bf16x8*>(bl + a * 256);
            };
            auto mma = [&](auto sc_) {
                constexpr int s = decltype(sc_)::value;
#pragma unroll
                for (int b = 0; b < 5; ++b)
#pragma unroll
                    for (int a = 0; a < 6; ++a) acc[a][b] = MFMA(nf[s][a], mf[s][b], acc[a][b]);
#pragma unroll
                for (int i = 0; i < 11; ++i) { SGB(0x8, 2); SGB(0x100, 1); }
                SGB(0x8, 8);
            };
            compute_loop<10>(read, mma);
            f32x4* xin = reinterpret_cast<f32x4*>(shm + L_X) + wid * 768 + lane;
            f32x4* xout = reinterpret_cast<f32x4*>(shm + L_X) + (wid ^ 2) * 768 + lane;
            float* uv = reinterpret_cast<float*>(shm + L_D_A);
            auto epi = [&](auto khc) {
                constexpr int KH = decltype(khc)::value;
#pragma unroll
                for (int b2 = 0; b2 < 2; ++b2)
#pragma unroll
                    for (int a = 0; a < 6; ++a) xout[(b2 * 6 + a) * 64] = acc[a][2 * (1 - KH) + b2];
                if (fr < 2) {
#pragma unroll
                    for (int a = 0; a < 6; ++a) *reinterpret_cast<f32x4*>(uv + (KH * 2 + fr) * 192 + 96 * nh + 16 * a + 4 * g) = acc[a][4];
                }
                BAR_LGKM();
                f32x2 rs2[2];
#pragma unroll
                for (int b2 = 0; b2 < 2; ++b2) rs2[b2] = *reinterpret_cast<const f32x2*>(shm + L_RS + (16 * (2 * KH + b2) + fr) * 8);
#pragma unroll
                for (int a = 0; a < 6; ++a) {
                    const float* up = uv + 96 * nh + 16 * a + 4 * g;
                    const f32x4 u4 = *reinterpret_cast<const f32x4*>(up) + *reinterpret_cast<const f32x4*>(up + 384);
                    const f32x4 v4 = *reinterpret_cast<const f32x4*>(up + 192) + *reinterpret_cast<const f32x4*>(up + 576);
                    const int f = cb * 192 + 96 * nh + 16 * a + 4 * g;
#pragma unroll
                    for (int b2 = 0; b2 < 2; ++b2) {
                        const int t = t0 + 16 * (2 * KH + b2) + fr;
                        f32x4 gsum = acc[a][2 * KH + b2] + xin[(b2 * 6 + a) * 64];
                        f32x4 vv = gsum * rs2[b2][0] + (u4 * rs2[b2][1] + v4);
                        u32x2 st2 = {pk2(fmaxf(vv[0], 0.f), fmaxf(vv[1], 0.f)), pk2(fmaxf(vv[2], 0.f), fmaxf(vv[3], 0.f))};
                        __builtin_amdgcn_raw_buffer_store_b64(st2, wrs, WS_HH + ((f >> 3) * S_ + t) * 16 + (f & 7) * 2, 0, 16);
                    }
                }
            };
            if (kh) epi(IC<1>{}); else epi(IC<0>{});
            WAIT_V(0);
            if (lane == 0) flag_set(ws, WS_FG + (2 * 32 + rb) * 256 + (cb * 4 + wid) * 8, tag);
            BAR_ONLY();
        }
        {
            const int moff = L_C_A + (u * 64 + 32 * mh + fr) * 16;
            const int noff = L_C_B + (u * 80 + fr) * 16;
            f32x4 acc[5][2];
#pragma unroll
            for (int a = 0; a < 5; ++a)
#pragma unroll
                for (int b = 0; b < 2; ++b) acc[a][b] = f32x4{0.f, 0.f, 0.f, 0.f};
            bf16x8 mf[2][2][2], nf[2][2][5];
            auto read = [&](int st, auto sc_) {
                constexpr int s = decltype(sc_)::value;
                const char* al = shm + moff + (st % 3) * 16384;
                const char* bl = shm + noff + (st % 3) * 20480;
#pragma unroll
                for (int k = 0; k < 2; ++k) {
#pragma unroll
                    for (int b = 0; b < 2; ++b) mf[s][k][b] = *reinterpret_cast<const bf16x8*>(al + k * 8192 + b * 256);
#pragma unroll
                    for (int a = 0; a < 5; ++a) nf[s][k][a] = *reinterpret_cast<const bf16x8*>(bl + k * 10240 + a * 256);
                }
            };
            auto mma = [&](auto sc_) {
                constexpr int s = decltype(sc_)::value;
#pragma unroll
                for (int k = 0; k < 2; ++k)
#pragma unroll
                    for (int b = 0; b < 2; ++b)
#pragma unroll
                        for (int a = 0; a < 5; ++a) acc[a][b] = MFMA(nf[s][k][a], mf[s][k][b], acc[a][b]);
#pragma unroll
                for (int i = 0; i < 14; ++i) { SGB(0x8, 1); SGB(0x100, 1); }
                SGB(0x8, 6);
            };
            compute_loop<12>(read, mma);
            f32x4* xin = reinterpret_cast<f32x4*>(shm + L_X) + wid * 320 + lane;
            f32x4* xout = reinterpret_cast<f32x4*>(shm + L_X) + (wid ^ 2) * 320 + lane;
            auto epi = [&](auto khc) {
                constexpr int KH = decltype(khc)::value;
#pragma unroll
                for (int a = 0; a < 5; ++a) xout[a * 64] = acc[a][1 - KH];
                BAR_LGKM();
#pragma unroll
                for (int a = 0; a < 5; ++a) {
                    f32x4 o = xin[a * 64];
                    f32x4 m = acc[a][KH];
                    f32x4 gg = *reinterpret_cast<const f32x4*>(tab + T_GB + 16 * a + 4 * g);
                    f32x4 y = m + o;
                    f32x2 v0 = up2(rx[a][0]) + bfr2(f32x2{gg[0], gg[1]} * bfr2(f32x2{y[0], y[1]}));
                    f32x2 v1 = up2(rx[a][1]) + bfr2(f32x2{gg[2], gg[3]} * bfr2(f32x2{y[2], y[3]}));
                    float vv[4] = {v0[0], v0[1], v1[0], v1[1]};
                    u32x2 st2 = {pk2(vv[0], vv[1]), pk2(vv[2], vv[3])};
                    *reinterpret_cast<u32x2*>(outp + (size_t)trow * D_ + n0 + 16 * a + 4 * g) = st2;
                }
            };
            if (kh) epi(IC<1>{}); else epi(IC<0>{});
        }
    }
}

extern "C" void kernel_launch(void* const* d_in, const int* in_sizes, int n_in, void* d_out, int out_size,
                              void* d_ws, size_t ws_size, hipStream_t stream) {
    k0_prep<<<256, 768, 0, stream>>>((const u16*)d_in[0], (const u16*)d_in[1], (const u16*)d_in[2], (const u16*)d_in[6],
                                     (const u16*)d_in[7], (const u16*)d_in[8], (const u16*)d_in[9], (char*)d_ws);
    k1_chain<<<256, 512, 0, stream>>>((const u16*)d_in[0], (const u16*)d_in[1], (const u16*)d_in[2],
                                      (const u16*)d_in[3], (const u16*)d_in[4], (u16*)d_out, (char*)d_ws);
}
```
